```python
import torch

def _softmax(x, axis=-1):
    return torch.softmax(x, dim=axis)
def _gelu(x):
    return torch.nn.functional.gelu(x, approximate="tanh")
def _silu(x):
    return torch.nn.functional.silu(x)
def _sigmoid(x):
    return torch.sigmoid(x)
def _layernorm(x, axis=-1, eps=1e-5):
    m = x.mean(dim=axis, keepdim=True)
    v = x.var(dim=axis, keepdim=True, unbiased=False)
    return (x - m) / torch.sqrt(v + eps)
def _scatter_add(tgt, idx, src, axis=0):
    return tgt.clone().index_add_(axis, idx, src)
def _segment_sum(src, seg, n):
    out = torch.zeros((n,) + src.shape[1:], dtype=src.dtype)
    return out.index_add_(0, seg, src)
def _pad(x, pw):
    flat = tuple(v for pair in reversed(pw) for v in pair)
    return torch.nn.functional.pad(x, flat)

kernel_name = 'concat_heads__grouped_kv_mqa__sliding_local_dilated__prompt'

def setup_inputs():

    _w = (lambda a: (lambda t: t.to(torch.bfloat16) if t.is_floating_point() else t)(torch.as_tensor(a)))
    def _A_setup():
        B, S, D, H = 1, 2048, 448, 16
        Dh = D // H
        return {
            "x": _w((lambda *s: torch.randn(*s, dtype=torch.bfloat16) * 0.05)(B, S, D)),
            "wq": _w((lambda *s: torch.randn(*s, dtype=torch.bfloat16) * 3.2)(D, H * Dh)),
            "wk": _w((lambda *s: torch.randn(*s, dtype=torch.bfloat16) * 3.2)(D, Dh)),
            "wv": _w((lambda *s: torch.randn(*s, dtype=torch.bfloat16) * 1.6)(D, Dh)),
            "wo": _w((lambda *s: torch.randn(*s, dtype=torch.bfloat16) * 1.6)(H * Dh, D)),
        }
    _A_in = _A_setup()

    def _B_setup():
        B, S, D = 1, 2048, 448
        return {
            "x": _w((lambda *s: torch.randn(*s, dtype=torch.bfloat16) * 0.1)(B, S, D)),
            "wqkv": _w((lambda *s: torch.randn(*s, dtype=torch.bfloat16) * 0.8)(D, 3 * D)),
            "wo": _w((lambda *s: torch.randn(*s, dtype=torch.bfloat16) * 0.8)(D, D)),
        }
    _B_in = _B_setup()

    return {
        "A_x": _A_in["x"],
        "A_wq": _A_in["wq"],
        "A_wk": _A_in["wk"],
        "A_wv": _A_in["wv"],
        "A_wo": _A_in["wo"],
        "B_x": _B_in["x"],
        "B_wqkv": _B_in["wqkv"],
        "B_wo": _B_in["wo"],
    }

def reference(A_x, A_wq, A_wk, A_wv, A_wo, B_x, B_wqkv, B_wo):
    def _A_ref(x, wq, wk, wv, wo):
        B, S, D = x.shape
        H = 16
        Dh = D // H
        q = (lambda x, *d: x.permute(*d))((x @ wq).reshape(B, S, H, Dh), 0, 2, 1, 3)
        k = x @ wk
        v = x @ wv
        scores = torch.einsum("bhqd,bkd->bhqk", q, k) * (Dh ** -0.5)
        p = _softmax(scores, axis=-1)
        o = torch.einsum("bhqk,bkd->bhqd", p, v)
        return (lambda x, *d: x.permute(*d))(o, 0, 2, 1, 3).reshape(B, S, H * Dh) @ wo

    def _B_ref(x, wqkv, wo):
        B, S, D = x.shape
        H = 16; Dh = D // H
        W, dil = 8, 2
        qkv = (lambda x, *d: x.permute(*d))((x @ wqkv).reshape(B, S, 3, H, Dh), 2, 0, 3, 1, 4)
        q, k, v = qkv[0], qkv[1], qkv[2]
        scale = Dh ** -0.5
        pos = torch.arange(S)
        off = torch.arange(W) * dil
        idx = pos[:, None] - off[None, :]
        mask = idx >= 0
        idx_c = torch.where(mask, idx, 0).reshape(-1)
        kg = k[:, :, idx_c, :].reshape(B, H, S, W, Dh)
        vg = v[:, :, idx_c, :].reshape(B, H, S, W, Dh)
        logits = torch.einsum("bhsd,bhswd->bhsw", q, kg) * scale
        logits = torch.where(mask[None, None, :, :], logits, -1e9)
        attn = _softmax(logits, axis=-1)
        o = torch.einsum("bhsw,bhswd->bhsd", attn, vg)
        return (lambda x, *d: x.permute(*d))(o, 0, 2, 1, 3).reshape(B, S, D) @ wo

    a = _A_ref(A_x, A_wq, A_wk, A_wv, A_wo)
    b = _B_ref(B_x, B_wqkv, B_wo)
    return torch.concatenate([a, b], axis=-1)
```

```cpp
#include <hip/hip_runtime.h>
#include <hip/hip_bf16.h>
#include <stdint.h>
#define MIMIC 0
typedef unsigned short u16;
__device__ __forceinline__ float bf2f(u16 b){ return __uint_as_float(((unsigned)b)<<16); }
__device__ __forceinline__ u16 f2bf(float f){ unsigned u=__float_as_uint(f); u += 0x7fffu + ((u>>16)&1u); return (u16)(u>>16); }
__device__ __forceinline__ float rbf(float f){ return bf2f(f2bf(f)); }
constexpr int S=2048, D=448, H=16, DH=28;

__global__ void gemm_naive(const u16* x, const u16* w, u16* out, int M, int N, int K, int ldo){
  int c = blockIdx.x*blockDim.x+threadIdx.x; int r = blockIdx.y;
  if(c>=N) return;
  float acc=0.f;
  for(int k=0;k<K;k++) acc += bf2f(x[r*K+k])*bf2f(w[k*N+c]);
  out[(size_t)r*ldo+c]=f2bf(acc);
}
__global__ void attnA(const u16* q, const u16* k, const u16* v, u16* o){
  int r=blockIdx.x, h=blockIdx.y; int t=threadIdx.x;
  __shared__ float sc[S]; __shared__ float red[256]; __shared__ float qq[DH];
  if(t<DH) qq[t]=bf2f(q[r*D+h*DH+t]);
  __syncthreads();
  const float scale = 0.18898223650461362f;
  float m=-1e30f;
  for(int j=t;j<S;j+=256){ float a=0.f; for(int d=0;d<DH;d++) a+=qq[d]*bf2f(k[j*DH+d]);
    a = a*scale;
    sc[j]=a; m=fmaxf(m,a);}
  red[t]=m; __syncthreads();
  for(int s=128;s>0;s>>=1){ if(t<s) red[t]=fmaxf(red[t],red[t+s]); __syncthreads(); }
  m=red[0]; __syncthreads();
  float l=0.f;
  for(int j=t;j<S;j+=256){ float e=expf(sc[j]-m); sc[j]=e; l+=e; }
  red[t]=l; __syncthreads();
  for(int s=128;s>0;s>>=1){ if(t<s) red[t]+=red[t+s]; __syncthreads(); }
  l=red[0]; __syncthreads();
  if(t<DH){ float a=0.f; for(int j=0;j<S;j++){ float p=sc[j]/l;
      a+=p*bf2f(v[j*DH+t]); }
    o[r*D+h*DH+t]=f2bf(a); }
}
__global__ void attnB(const u16* qkv, u16* o){
  int idx = blockIdx.x*blockDim.x+threadIdx.x;
  if(idx>=S*H) return;
  int r=idx/H, h=idx%H;
  const float scale = 0.18898223650461362f;
  float lg[8]; float m=-1e30f;
  for(int w=0;w<8;w++){ int j=r-2*w; float a;
    if(j>=0){ a=0.f; for(int d=0;d<DH;d++) a+=bf2f(qkv[r*1344+h*DH+d])*bf2f(qkv[j*1344+448+h*DH+d]);
      a=a*scale;
    } else a = MIMIC ? rbf(-1e9f) : -1e9f;
    lg[w]=a; m=fmaxf(m,a);}
  float l=0.f; for(int w=0;w<8;w++){ lg[w]=expf(lg[w]-m); l+=lg[w]; }
  for(int d=0;d<DH;d++){ float a=0.f; for(int w=0;w<8;w++){ int j=r-2*w; if(j<0) j=0; float p=lg[w]/l;
      a+=p*bf2f(qkv[j*1344+896+h*DH+d]); }
    o[r*D+h*DH+d]=f2bf(a);}
}
extern "C" void kernel_launch(void* const* d_in, const int* in_sizes, int n_in, void* d_out, int out_size, void* d_ws, size_t ws_size, hipStream_t stream){
  const u16 *Ax=(const u16*)d_in[0], *Awq=(const u16*)d_in[1], *Awk=(const u16*)d_in[2], *Awv=(const u16*)d_in[3], *Awo=(const u16*)d_in[4];
  const u16 *Bx=(const u16*)d_in[5], *Bw=(const u16*)d_in[6], *Bwo=(const u16*)d_in[7];
  u16* out=(u16*)d_out; u16* ws=(u16*)d_ws;
  u16* q=ws; u16* k=q+S*D; u16* v=k+S*DH; u16* oa=v+S*DH; u16* qkv=oa+S*D; u16* ob=qkv+S*1344;
  gemm_naive<<<dim3(2,S),256,0,stream>>>(Ax,Awq,q,S,448,448,448);
  gemm_naive<<<dim3(1,S),64,0,stream>>>(Ax,Awk,k,S,28,448,28);
  gemm_naive<<<dim3(1,S),64,0,stream>>>(Ax,Awv,v,S,28,448,28);
  gemm_naive<<<dim3(6,S),256,0,stream>>>(Bx,Bw,qkv,S,1344,448,1344);
  attnA<<<dim3(S,H),256,0,stream>>>(q,k,v,oa);
  attnB<<<(S*H+255)/256,256,0,stream>>>(qkv,ob);
  gemm_naive<<<dim3(2,S),256,0,stream>>>(oa,Awo,out,S,448,448,896);
  gemm_naive<<<dim3(2,S),256,0,stream>>>(ob,Bwo,out+448,S,448,448,896);
}
```

```cpp
#include <hip/hip_runtime.h>
#include <stdint.h>

typedef unsigned short u16;
using bf16x8 = __attribute__((ext_vector_type(8))) short;
using s16x4  = __attribute__((ext_vector_type(4))) short;
using f32x16 = __attribute__((ext_vector_type(16))) float;
using u32x4  = __attribute__((ext_vector_type(4))) unsigned;
using u32x2  = __attribute__((ext_vector_type(2))) unsigned;
typedef __attribute__((address_space(3))) const char* lds_cptr;
typedef short v4i16_t __attribute__((ext_vector_type(4)));
#define DI __device__ __forceinline__
#define MFMA(a, b, c) __builtin_amdgcn_mfma_f32_32x32x16_bf16((a), (b), (c), 0, 0, 0)

constexpr int S = 2048, DM = 448, NH = 16, DH = 28;
constexpr float SCALE = 0.18898223650461362f;
constexpr float QSCALE = SCALE * 1.4426950408889634f;

constexpr size_t WS_Q   = 0;
constexpr size_t WS_KV  = WS_Q + (size_t)NH * S * 32 * 2;
constexpr size_t WS_QKV = WS_KV + (size_t)64 * 4096;
constexpr size_t WS_O   = WS_QKV + (size_t)S * 1344 * 2 + 4096;

typedef __bf16 bf16x2_t __attribute__((ext_vector_type(2)));
typedef float f32x2_t __attribute__((ext_vector_type(2)));
DI unsigned cvtpk(float lo, float hi) { f32x2_t v = {lo, hi}; return __builtin_bit_cast(unsigned, __builtin_convertvector(v, bf16x2_t)); }
DI void glds16(const void* g, unsigned lds_base) {
  unsigned sv; asm volatile("s_mov_b32 %0, m0\n\ts_mov_b32 m0, %2\n\ts_nop 0\n\tglobal_load_lds_dwordx4 %1, off\n\ts_mov_b32 m0, %0"
                            : "=&s"(sv) : "v"(g), "s"(lds_base) : "memory"); }
DI void glds4(const void* g, unsigned lds_base) {
  unsigned sv; asm volatile("s_mov_b32 %0, m0\n\ts_mov_b32 m0, %2\n\ts_nop 0\n\tglobal_load_lds_dword %1, off\n\ts_mov_b32 m0, %0"
                            : "=&s"(sv) : "v"(g), "s"(lds_base) : "memory"); }
template <int N> DI void wait_bar() { asm volatile("s_waitcnt vmcnt(%0) lgkmcnt(0)\n\ts_barrier" :: "i"(N) : "memory"); }
DI bf16x8 lds_rd128(lds_cptr p) { return *(const __attribute__((address_space(3))) bf16x8*)p; }
DI s16x4 lds_tr(lds_cptr p) { return __builtin_bit_cast(s16x4, __builtin_amdgcn_ds_read_tr16_b64_v4i16((__attribute__((address_space(3))) v4i16_t*)p)); }
DI unsigned sfl(unsigned x) { return (unsigned)__builtin_amdgcn_readfirstlane((int)x); }
template <int I> struct IC { static constexpr int value = I; };
template <int I, int N, class F> DI void static_for(F&& f) { if constexpr (I < N) { f(IC<I>{}); static_for<I + 1, N>(f); } }

constexpr int G_NBUF = 4;
template <int NP> struct GemmCfg { static constexpr int STAGE_BYTES = 16384 + NP * 4096; static constexpr int LDS = STAGE_BYTES * G_NBUF; };

struct GemmArgs {
  const u16* X; const u16* W; const u16* Wk; const u16* Wv; u16* out0; u16* out1;
};

template <int MODE, int NP>
DI void gemm_tile(char* lds, const u16* __restrict__ X, int ldx, int row0, const u16* __restrict__ W, int ldw, int col0, int ncols,
                  const u16* __restrict__ Wk, const u16* __restrict__ Wv, u16* __restrict__ out0, u16* __restrict__ out1) {
  constexpr int SB = GemmCfg<NP>::STAGE_BYTES;
  constexpr int NPW = NP / 2;
  constexpr int L = (MODE == 1) ? 7 : (2 + NP / 2);
  const int tid = threadIdx.x, lane = tid & 63, r32 = lane & 31, hi = lane >> 5;
  const int wid = (int)sfl(tid >> 6);
  const unsigned lds0 = (unsigned)(uintptr_t)lds;
  const u16* xsrc[2];
#pragma unroll
  for (int i = 0; i < 2; ++i) { const int row = (2 * wid + i) * 8 + (lane >> 3); const int c = (lane & 7) ^ ((row >> 1) & 7);
    xsrc[i] = X + (size_t)(row0 + row) * ldx + c * 8; }
  const u16* wsrc[2]; unsigned wdst[2];
  if constexpr (MODE == 1) {
    const int panel = wid >> 2, piece = wid & 3;
    wsrc[0] = W + (size_t)(piece * 16 + (lane >> 2)) * ldw + col0 + panel * 32 + (lane & 3) * 8;
    wdst[0] = 16384 + panel * 4096 + piece * 1024;
    wsrc[1] = nullptr; wdst[1] = 0;
  } else if constexpr (NP == 4) {
    const int panel = wid >> 1; int pc = col0 + panel * 32; if (pc > ncols - 32) pc = ncols - 32;
#pragma unroll
    for (int i = 0; i < 2; ++i) { const int piece = 2 * (wid & 1) + i;
      wsrc[i] = W + (size_t)(piece * 16 + (lane >> 2)) * ldw + pc + (lane & 3) * 8; wdst[i] = 16384 + panel * 4096 + piece * 1024; }
  } else {
    const int panel = wid >> 2, piece = wid & 3;
    wsrc[0] = W + (size_t)(piece * 16 + (lane >> 2)) * ldw + col0 + panel * 32 + (lane & 3) * 8;
    wdst[0] = 16384 + panel * 4096 + piece * 1024;
    wsrc[1] = nullptr; wdst[1] = 0;
  }
  const int kvoff = (lane >> 4) * 28 + ((lane & 15) < 14 ? (lane & 15) : 13) * 2;

  auto issue = [&](int s) {
    const unsigned base = lds0 + (unsigned)((s % G_NBUF) * SB);
    glds16(xsrc[0] + s * 64, sfl(base + (2 * wid) * 1024));
    glds16(xsrc[1] + s * 64, sfl(base + (2 * wid + 1) * 1024));
    glds16(wsrc[0] + (size_t)s * 64 * ldw, sfl(base + wdst[0]));
    if constexpr (MODE != 1 && NP == 4) glds16(wsrc[1] + (size_t)s * 64 * ldw, sfl(base + wdst[1]));
    if constexpr (MODE == 1) {
#pragma unroll
      for (int i = 0; i < 2; ++i) { const int piece = 2 * wid + i;
        glds4(Wk + (s * 64 + piece * 4) * 28 + kvoff, sfl(base + 16384 + 2 * 4096 + piece * 256));
        glds4(Wv + (s * 64 + piece * 4) * 28 + kvoff, sfl(base + 16384 + 3 * 4096 + piece * 256)); }
    }
  };

  const int wr = wid & 3, wc = wid >> 2;
  const int xrow = wr * 32 + r32; const int xsw = (xrow >> 1) & 7;
  const int g16 = lane >> 4, i16 = lane & 15;
  const int woff = 16384 + (wc * NPW) * 4096 + (8 * (g16 >> 1) + (i16 >> 2)) * 64 + (g16 & 1) * 32 + (i16 & 3) * 8;

  f32x16 acc[NPW];
#pragma unroll
  for (int p = 0; p < NPW; ++p) acc[p] = f32x16{};

  issue(0); issue(1); issue(2);
  static_for<0, 7>([&](auto sc) {
    constexpr int s = decltype(sc)::value;
    constexpr int ahead = (s + 2 < 6 ? s + 2 : 6) - s;
    wait_bar<ahead * L>();
    if constexpr (s + 3 <= 6) issue(s + 3);
    const lds_cptr b = (lds_cptr)lds + (s % G_NBUF) * SB;
#pragma unroll
    for (int ks = 0; ks < 4; ++ks) {
      const bf16x8 xf = lds_rd128(b + xrow * 128 + (((2 * ks + hi) ^ xsw) * 16));
#pragma unroll
      for (int p = 0; p < NPW; ++p) {
        const s16x4 lo = lds_tr(b + woff + p * 4096 + ks * 1024), hh = lds_tr(b + woff + p * 4096 + ks * 1024 + 256);
        const bf16x8 wf = __builtin_shufflevector(lo, hh, 0, 1, 2, 3, 4, 5, 6, 7);
        if (MODE == 1 && p == 1 && wc == 1) acc[p] = MFMA(xf, wf, acc[p]);
        else acc[p] = MFMA(wf, xf, acc[p]);
      }
    }
  });

  const int row = row0 + xrow;
#pragma unroll
  for (int p = 0; p < NPW; ++p) {
    const int panel = wc * NPW + p;
    if (MODE == 1 && panel >= 2) {
      const int tile = (row0 >> 5) + wr; u16* kv = out1 + (size_t)tile * 2048;
      if (panel == 2) {
#pragma unroll
        for (int g = 0; g < 4; ++g) { u32x2 v;
          if (g == 3 && hi == 1) v = u32x2{0u, 0u};
          else v = u32x2{cvtpk(acc[p][4 * g], acc[p][4 * g + 1]), cvtpk(acc[p][4 * g + 2], acc[p][4 * g + 3])};
          *(u32x2*)(kv + (g >> 1) * 512 + (r32 + 32 * (g & 1)) * 8 + 4 * hi) = v; }
      } else {
#pragma unroll
        for (int g = 0; g < 4; ++g) { u32x2 v = u32x2{cvtpk(acc[p][4 * g], acc[p][4 * g + 1]), cvtpk(acc[p][4 * g + 2], acc[p][4 * g + 3])};
          if (r32 == 28) v = u32x2{0x3F803F80u, 0x3F803F80u}; else if (r32 > 28) v = u32x2{0u, 0u};
          *(u32x2*)(kv + 1024 + (g >> 1) * 512 + lane * 8 + 4 * (g & 1)) = v; }
      }
    } else if (MODE == 0 || MODE == 1) {
#pragma unroll
      for (int g = 0; g < 4; ++g) { const int c = col0 + panel * 32 + 8 * g + 4 * hi; const int head = c / 28, d = c - head * 28;
        u32x2 v = u32x2{cvtpk(acc[p][4 * g] * QSCALE, acc[p][4 * g + 1] * QSCALE), cvtpk(acc[p][4 * g + 2] * QSCALE, acc[p][4 * g + 3] * QSCALE)};
        u16* dst = out0 + ((size_t)head * S + row) * 32 + d;
        if (d == 24) *(u32x4*)dst = u32x4{v[0], v[1], 0u, 0u}; else *(u32x2*)dst = v; }
    } else if (MODE == 2) {
      if (col0 + panel * 32 < 1344) {
#pragma unroll
        for (int g = 0; g < 4; ++g) { const int c = col0 + panel * 32 + 8 * g + 4 * hi;
          *(u32x2*)(out0 + (size_t)row * 1344 + c) = u32x2{cvtpk(acc[p][4 * g], acc[p][4 * g + 1]), cvtpk(acc[p][4 * g + 2], acc[p][4 * g + 3])}; }
      }
    } else {
#pragma unroll
      for (int g = 0; g < 4; ++g) { const int c = col0 + panel * 32 + 8 * g + 4 * hi;
        *(u32x2*)(out0 + (size_t)row * 896 + c) = u32x2{cvtpk(acc[p][4 * g], acc[p][4 * g + 1]), cvtpk(acc[p][4 * g + 2], acc[p][4 * g + 3])}; }
    }
  }
}

__global__ __launch_bounds__(512, 2) void k1_proj(const u16* __restrict__ Ax, const u16* __restrict__ Awq, const u16* __restrict__ Awk, const u16* __restrict__ Awv,
                                                  const u16* __restrict__ Bx, const u16* __restrict__ Bw, char* __restrict__ ws) {
  extern __shared__ __attribute__((aligned(16))) char lds[];
  const int b = blockIdx.x, xcd = b & 7, j = b >> 3; const int rt = 2 * xcd + (j & 1), ct = j >> 1; const int row0 = rt * 128;
  u16* Qs = (u16*)(ws + WS_Q); u16* KV = (u16*)(ws + WS_KV); u16* QKV = (u16*)(ws + WS_QKV);
  if (ct < 3) gemm_tile<0, 4>(lds, Ax, DM, row0, Awq, DM, ct * 128, DM, nullptr, nullptr, Qs, nullptr);
  else if (ct == 3) gemm_tile<1, 4>(lds, Ax, DM, row0, Awq, DM, 384, DM, Awk, Awv, Qs, KV);
  else gemm_tile<2, 4>(lds, Bx, DM, row0, Bw, 1344, (ct - 4) * 128, 1344, nullptr, nullptr, QKV, nullptr);
}

__global__ __launch_bounds__(512, 2) void k3_out(const char* __restrict__ ws, const u16* __restrict__ Awo, const u16* __restrict__ Bwo, u16* __restrict__ out) {
  extern __shared__ __attribute__((aligned(16))) char lds[];
  const int b = blockIdx.x, xcd = b & 7, j = b >> 3; const int rt = 2 * xcd + (j & 1), ct = j >> 1; const int row0 = rt * 128;
  const u16* O = (const u16*)(ws + WS_O);
  const int part = ct >= 7 ? 1 : 0; const int c0 = (ct - 7 * part) * 64;
  gemm_tile<3, 2>(lds, O + part * 448, 896, row0, part ? Bwo : Awo, DM, c0, DM, nullptr, nullptr, out + part * 448, nullptr);
}

constexpr int A_STAGE = 16384;
constexpr int A_LDS_SIDE = 3 * A_STAGE;
constexpr int A_LDS_MRG = A_LDS_SIDE + 65536;
constexpr int A_LDS = A_LDS_MRG + 4 * 17 * 256;
constexpr float THR = 30.f;

DI float max16(const f32x16& s) {
  float a = __builtin_fmaxf(__builtin_fmaxf(s[0], s[1]), s[2]), b = __builtin_fmaxf(__builtin_fmaxf(s[3], s[4]), s[5]);
  a = __builtin_fmaxf(__builtin_fmaxf(a, s[6]), s[7]); b = __builtin_fmaxf(__builtin_fmaxf(b, s[8]), s[9]);
  a = __builtin_fmaxf(__builtin_fmaxf(a, s[10]), s[11]); b = __builtin_fmaxf(__builtin_fmaxf(b, s[12]), s[13]);
  a = __builtin_fmaxf(__builtin_fmaxf(a, s[14]), s[15]);
  return __builtin_fmaxf(a, b); }
DI float xhalf_max(float m) { auto rr = __builtin_amdgcn_permlane32_swap(__float_as_uint(m), __float_as_uint(m), false, false);
  return __builtin_fmaxf(__uint_as_float(rr[0]), __uint_as_float(rr[1])); }

__global__ __launch_bounds__(512, 2) void k2_attn(char* __restrict__ ws) {
  extern __shared__ __attribute__((aligned(16))) char lds[];
  const int tid = threadIdx.x, lane = tid & 63, r32 = lane & 31, hi = lane >> 5;
  const int wid = (int)sfl(tid >> 6), qg = wid & 3, kh = wid >> 2;
  const int b = blockIdx.x, xcd = b & 7, j = b >> 3;
  const int rc = xcd * 8 + (j >> 2), hg = j & 3;
  const int head = hg * 4 + qg, row0 = rc * 32;
  const u16* Qs = (const u16*)(ws + WS_Q); const char* KV = ws + WS_KV; const char* QKV = ws + WS_QKV; u16* O = (u16*)(ws + WS_O);
  const unsigned lds0 = (unsigned)(uintptr_t)lds;

  const u16* qp = Qs + ((size_t)head * S + row0 + r32) * 32 + hi * 8;
  const bf16x8 q0 = *(const bf16x8*)qp, q1 = *(const bf16x8*)(qp + 16);

  const char* kvsrc = KV + (size_t)(kh * 32) * 4096 + (2 * qg) * 1024 + lane * 16;
  const unsigned kvdst = kh * 8192 + (2 * qg) * 1024;
  auto issue_kv = [&](int s, int buf) { const unsigned d = sfl(lds0 + buf * A_STAGE + kvdst);
    glds16(kvsrc + (size_t)s * 8192, d); glds16(kvsrc + (size_t)s * 8192 + 1024, d + 1024); };
  const int brow0 = (xcd * 32 + j) * 8; const int bstart = brow0 >= 14 ? brow0 - 14 : 0;
  const char* sdsrc = QKV + (size_t)bstart * 2688 + wid * 1024 + lane * 16;
  auto issue_side = [&](int s) { glds16(sdsrc + s * 8192, sfl(lds0 + A_LDS_SIDE + s * 8192 + wid * 1024)); };

  issue_kv(0, 0); issue_kv(1, 1);

  f32x16 o = f32x16{}, negm;
  const lds_cptr rd0 = (lds_cptr)lds + kh * 8192 + lane * 16;
  int buf = 0, nbuf = 2;

  auto tile = [&](lds_cptr p, bool first) {
    const bf16x8 kf0 = lds_rd128(p), kf1 = lds_rd128(p + 1024), vf0 = lds_rd128(p + 2048), vf1 = lds_rd128(p + 3072);
    f32x16 s;
    if (first) {
      s = MFMA(kf0, q0, f32x16{}); s = MFMA(kf1, q1, s);
      const float m = xhalf_max(max16(s));
#pragma unroll
      for (int r = 0; r < 16; ++r) { s[r] -= m; negm[r] = -m; }
    } else {
      s = MFMA(kf0, q0, negm); s = MFMA(kf1, q1, s);
      const float tm = max16(s);
      if (__builtin_expect(__any(tm > THR), 0)) {
        const float dl = __builtin_fmaxf(xhalf_max(tm), 0.f); const float al = __builtin_amdgcn_exp2f(-dl);
#pragma unroll
        for (int r = 0; r < 16; ++r) { s[r] -= dl; negm[r] -= dl; o[r] *= al; }
      }
    }
    u32x4 p0, p1;
#pragma unroll
    for (int i = 0; i < 4; ++i) { p0[i] = cvtpk(__builtin_amdgcn_exp2f(s[2 * i]), __builtin_amdgcn_exp2f(s[2 * i + 1]));
                                  p1[i] = cvtpk(__builtin_amdgcn_exp2f(s[8 + 2 * i]), __builtin_amdgcn_exp2f(s[8 + 2 * i + 1])); }
    o = MFMA(vf0, __builtin_bit_cast(bf16x8, p0), o); o = MFMA(vf1, __builtin_bit_cast(bf16x8, p1), o);
  };

  wait_bar<2>();
  issue_side(0); issue_kv(2, 2);
  tile(rd0, true); tile(rd0 + 4096, false);
  buf = 1; nbuf = 0;
  for (int s = 1; s < 14; ++s) {
    wait_bar<2>();
    if (s < 8) issue_side(s);
    issue_kv(s + 2, nbuf);
    const lds_cptr p = rd0 + buf * A_STAGE;
    tile(p, false); tile(p + 4096, false);
    buf = buf == 2 ? 0 : buf + 1; nbuf = nbuf == 2 ? 0 : nbuf + 1;
  }
  wait_bar<2>();
  { const lds_cptr p = rd0 + buf * A_STAGE; tile(p, false); tile(p + 4096, false); buf = buf == 2 ? 0 : buf + 1; }
  wait_bar<0>();
  { const lds_cptr p = rd0 + buf * A_STAGE; tile(p, false); tile(p + 4096, false); }

  float* mrg = (float*)(lds + A_LDS_MRG) + qg * 17 * 64;
  if (kh == 1) {
#pragma unroll
    for (int r = 0; r < 16; ++r) mrg[r * 64 + lane] = o[r];
    mrg[16 * 64 + lane] = negm[0];
  }
  __syncthreads();
  if (kh == 0) {
    const float nb = mrg[16 * 64 + lane], na = negm[0];
    const float nm = __builtin_fminf(na, nb);
    const float fa = __builtin_amdgcn_exp2f(nm - na), fb = __builtin_amdgcn_exp2f(nm - nb);
#pragma unroll
    for (int r = 0; r < 16; ++r) o[r] = o[r] * fa + mrg[r * 64 + lane] * fb;
    const float l = __shfl(o[12], r32 + 32, 64);
    const float il = 1.0f / l;
    u16* dst = O + (size_t)(row0 + r32) * 896 + head * 28 + 4 * hi;
#pragma unroll
    for (int g = 0; g < 4; ++g) if (!(g == 3 && hi == 1))
      *(u32x2*)(dst + 8 * g) = u32x2{cvtpk(o[4 * g] * il, o[4 * g + 1] * il), cvtpk(o[4 * g + 2] * il, o[4 * g + 3] * il)};
  } else {
    const int t = tid - 256, pr = t >> 1, hf = t & 1, rl = pr >> 4, hb = pr & 15; const int r = brow0 + rl;
    const char* img = lds + A_LDS_SIDE + hb * 56 + hf * 28;
    const unsigned* qv = (const unsigned*)(img + (r - bstart) * 2688);
    float qf[14];
#pragma unroll
    for (int i = 0; i < 7; ++i) { const unsigned u = qv[i]; qf[2 * i] = __uint_as_float(u << 16); qf[2 * i + 1] = __uint_as_float(u & 0xffff0000u); }
    float lg[8]; float m = -3.0e38f;
#pragma unroll
    for (int w = 0; w < 8; ++w) { const int jr = r - 2 * w; const int jc = jr >= 0 ? jr : bstart;
      const unsigned* kp = (const unsigned*)(img + (jc - bstart) * 2688 + 896); float a = 0.f;
#pragma unroll
      for (int i = 0; i < 7; ++i) { const unsigned u = kp[i]; a = __builtin_fmaf(qf[2 * i], __uint_as_float(u << 16), a); a = __builtin_fmaf(qf[2 * i + 1], __uint_as_float(u & 0xffff0000u), a); }
      a += __shfl_xor(a, 1, 64);
      lg[w] = jr >= 0 ? a * SCALE : -1e9f; m = __builtin_fmaxf(m, lg[w]); }
    float l = 0.f;
#pragma unroll
    for (int w = 0; w < 8; ++w) { lg[w] = __builtin_amdgcn_exp2f((lg[w] - m) * 1.4426950408889634f); l += lg[w]; }
    const float il = 1.0f / l; float oa[14];
#pragma unroll
    for (int i = 0; i < 14; ++i) oa[i] = 0.f;
#pragma unroll
    for (int w = 0; w < 8; ++w) { const int jr = r - 2 * w; const int jc = jr >= 0 ? jr : bstart; const float pw = lg[w] * il;
      const unsigned* vp = (const unsigned*)(img + (jc - bstart) * 2688 + 1792);
#pragma unroll
      for (int i = 0; i < 7; ++i) { const unsigned u = vp[i]; oa[2 * i] = __builtin_fmaf(pw, __uint_as_float(u << 16), oa[2 * i]); oa[2 * i + 1] = __builtin_fmaf(pw, __uint_as_float(u & 0xffff0000u), oa[2 * i + 1]); } }
    unsigned* dst = (unsigned*)(O + (size_t)r * 896 + 448 + hb * 28 + hf * 14);
#pragma unroll
    for (int i = 0; i < 7; ++i) dst[i] = cvtpk(oa[2 * i], oa[2 * i + 1]);
  }
}

extern "C" void kernel_launch(void* const* d_in, const int* in_sizes, int n_in, void* d_out, int out_size, void* d_ws, size_t ws_size, hipStream_t stream) {
  const u16 *Ax = (const u16*)d_in[0], *Awq = (const u16*)d_in[1], *Awk = (const u16*)d_in[2], *Awv = (const u16*)d_in[3], *Awo = (const u16*)d_in[4];
  const u16 *Bx = (const u16*)d_in[5], *Bw = (const u16*)d_in[6], *Bwo = (const u16*)d_in[7];
  char* ws = (char*)d_ws;
  (void)hipFuncSetAttribute((const void*)k1_proj, hipFuncAttributeMaxDynamicSharedMemorySize, GemmCfg<4>::LDS);
  (void)hipFuncSetAttribute((const void*)k2_attn, hipFuncAttributeMaxDynamicSharedMemorySize, A_LDS);
  (void)hipFuncSetAttribute((const void*)k3_out, hipFuncAttributeMaxDynamicSharedMemorySize, GemmCfg<2>::LDS);
  k1_proj<<<240, 512, GemmCfg<4>::LDS, stream>>>(Ax, Awq, Awk, Awv, Bx, Bw, ws);
  k2_attn<<<256, 512, A_LDS, stream>>>(ws);
  k3_out<<<224, 512, GemmCfg<2>::LDS, stream>>>(ws, Awo, Bwo, (u16*)d_out);
}
```

```cpp
#include <hip/hip_runtime.h>
#include <stdint.h>

typedef unsigned short u16;
using bf16x8 = __attribute__((ext_vector_type(8))) short;
using s16x4  = __attribute__((ext_vector_type(4))) short;
using f32x16 = __attribute__((ext_vector_type(16))) float;
using u32x4  = __attribute__((ext_vector_type(4))) unsigned;
using u32x2  = __attribute__((ext_vector_type(2))) unsigned;
typedef __attribute__((address_space(3))) const char* lds_cptr;
typedef short v4i16_t __attribute__((ext_vector_type(4)));
#define DI __device__ __forceinline__
#define MFMA(a, b, c) __builtin_amdgcn_mfma_f32_32x32x16_bf16((a), (b), (c), 0, 0, 0)

constexpr int S = 2048, DM = 448, NH = 16, DH = 28;
constexpr float SCALE = 0.18898223650461362f;
constexpr float QSCALE = SCALE * 1.4426950408889634f;

constexpr size_t WS_Q   = 0;
constexpr size_t WS_KV  = WS_Q + (size_t)NH * S * 32 * 2;
constexpr size_t WS_KN  = WS_KV + (size_t)64 * 4096;
constexpr size_t WS_QKV = WS_KN + 256;
constexpr size_t WS_O   = WS_QKV + (size_t)S * 1344 * 2 + 4096;

typedef __bf16 bf16x2_t __attribute__((ext_vector_type(2)));
typedef float f32x2_t __attribute__((ext_vector_type(2)));
DI unsigned cvtpk(float lo, float hi) { f32x2_t v = {lo, hi}; return __builtin_bit_cast(unsigned, __builtin_convertvector(v, bf16x2_t)); }
DI void glds16(const void* g, unsigned lds_base) {
  unsigned sv; asm volatile("s_mov_b32 %0, m0\n\ts_mov_b32 m0, %2\n\ts_nop 0\n\tglobal_load_lds_dwordx4 %1, off\n\ts_mov_b32 m0, %0"
                            : "=&s"(sv) : "v"(g), "s"(lds_base) : "memory"); }
DI void glds4(const void* g, unsigned lds_base) {
  unsigned sv; asm volatile("s_mov_b32 %0, m0\n\ts_mov_b32 m0, %2\n\ts_nop 0\n\tglobal_load_lds_dword %1, off\n\ts_mov_b32 m0, %0"
                            : "=&s"(sv) : "v"(g), "s"(lds_base) : "memory"); }
template <int N> DI void wait_bar() { asm volatile("s_waitcnt vmcnt(%0) lgkmcnt(0)\n\ts_barrier" :: "i"(N) : "memory"); }
DI bf16x8 lds_rd128(lds_cptr p) { return *(const __attribute__((address_space(3))) bf16x8*)p; }
DI s16x4 lds_tr(lds_cptr p) { return __builtin_bit_cast(s16x4, __builtin_amdgcn_ds_read_tr16_b64_v4i16((__attribute__((address_space(3))) v4i16_t*)p)); }
DI unsigned sfl(unsigned x) { return (unsigned)__builtin_amdgcn_readfirstlane((int)x); }
template <int I> struct IC { static constexpr int value = I; };
template <int I, int N, class F> DI void static_for(F&& f) { if constexpr (I < N) { f(IC<I>{}); static_for<I + 1, N>(f); } }

constexpr int G_NBUF = 4;
template <int NP> struct GemmCfg { static constexpr int STAGE_BYTES = 16384 + NP * 4096; static constexpr int LDS = STAGE_BYTES * G_NBUF; };

struct GemmArgs {
  const u16* X; const u16* W; const u16* Wk; const u16* Wv; u16* out0; u16* out1;
};

template <int MODE, int NP>
DI void gemm_tile(char* lds, const u16* __restrict__ X, int ldx, int row0, const u16* __restrict__ W, int ldw, int col0, int ncols,
                  const u16* __restrict__ Wk, const u16* __restrict__ Wv, u16* __restrict__ out0, u16* __restrict__ out1) {
  constexpr int SB = GemmCfg<NP>::STAGE_BYTES;
  constexpr int NPW = NP / 2;
  constexpr int L = (MODE == 1) ? 7 : (2 + NP / 2);
  const int tid = threadIdx.x, lane = tid & 63, r32 = lane & 31, hi = lane >> 5;
  const int wid = (int)sfl(tid >> 6);
  const unsigned lds0 = (unsigned)(uintptr_t)lds;
  const u16* xsrc[2];
#pragma unroll
  for (int i = 0; i < 2; ++i) { const int row = (2 * wid + i) * 8 + (lane >> 3); const int c = (lane & 7) ^ ((row >> 1) & 7);
    xsrc[i] = X + (size_t)(row0 + row) * ldx + c * 8; }
  const u16* wsrc[2]; unsigned wdst[2];
  if constexpr (MODE == 1) {
    const int panel = wid >> 2, piece = wid & 3;
    wsrc[0] = W + (size_t)(piece * 16 + (lane >> 2)) * ldw + col0 + panel * 32 + (lane & 3) * 8;
    wdst[0] = 16384 + panel * 4096 + piece * 1024;
    wsrc[1] = nullptr; wdst[1] = 0;
  } else if constexpr (NP == 4) {
    const int panel = wid >> 1; int pc = col0 + panel * 32; if (pc > ncols - 32) pc = ncols - 32;
#pragma unroll
    for (int i = 0; i < 2; ++i) { const int piece = 2 * (wid & 1) + i;
      wsrc[i] = W + (size_t)(piece * 16 + (lane >> 2)) * ldw + pc + (lane & 3) * 8; wdst[i] = 16384 + panel * 4096 + piece * 1024; }
  } else {
    const int panel = wid >> 2, piece = wid & 3;
    wsrc[0] = W + (size_t)(piece * 16 + (lane >> 2)) * ldw + col0 + panel * 32 + (lane & 3) * 8;
    wdst[0] = 16384 + panel * 4096 + piece * 1024;
    wsrc[1] = nullptr; wdst[1] = 0;
  }
  const int kvoff = (lane >> 4) * 28 + ((lane & 15) < 14 ? (lane & 15) : 13) * 2;

  auto issue = [&](int s) {
    const unsigned base = lds0 + (unsigned)((s % G_NBUF) * SB);
    glds16(xsrc[0] + s * 64, sfl(base + (2 * wid) * 1024));
    glds16(xsrc[1] + s * 64, sfl(base + (2 * wid + 1) * 1024));
    glds16(wsrc[0] + (size_t)s * 64 * ldw, sfl(base + wdst[0]));
    if constexpr (MODE != 1 && NP == 4) glds16(wsrc[1] + (size_t)s * 64 * ldw, sfl(base + wdst[1]));
    if constexpr (MODE == 1) {
#pragma unroll
      for (int i = 0; i < 2; ++i) { const int piece = 2 * wid + i;
        glds4(Wk + (s * 64 + piece * 4) * 28 + kvoff, sfl(base + 16384 + 2 * 4096 + piece * 256));
        glds4(Wv + (s * 64 + piece * 4) * 28 + kvoff, sfl(base + 16384 + 3 * 4096 + piece * 256)); }
    }
  };

  const int wr = wid & 3, wc = wid >> 2;
  const int xrow = wr * 32 + r32; const int xsw = (xrow >> 1) & 7;
  const int g16 = lane >> 4, i16 = lane & 15;
  const int woff = 16384 + (wc * NPW) * 4096 + (8 * (g16 >> 1) + (i16 >> 2)) * 64 + (g16 & 1) * 32 + (i16 & 3) * 8;

  f32x16 acc[NPW];
#pragma unroll
  for (int p = 0; p < NPW; ++p) acc[p] = f32x16{};

  issue(0); issue(1); issue(2);
  static_for<0, 7>([&](auto sc) {
    constexpr int s = decltype(sc)::value;
    constexpr int ahead = (s + 2 < 6 ? s + 2 : 6) - s;
    wait_bar<ahead * L>();
    if constexpr (s + 3 <= 6) issue(s + 3);
    const lds_cptr b = (lds_cptr)lds + (s % G_NBUF) * SB;
#pragma unroll
    for (int ks = 0; ks < 4; ++ks) {
      const bf16x8 xf = lds_rd128(b + xrow * 128 + (((2 * ks + hi) ^ xsw) * 16));
#pragma unroll
      for (int p = 0; p < NPW; ++p) {
        const s16x4 lo = lds_tr(b + woff + p * 4096 + ks * 1024), hh = lds_tr(b + woff + p * 4096 + ks * 1024 + 256);
        const bf16x8 wf = __builtin_shufflevector(lo, hh, 0, 1, 2, 3, 4, 5, 6, 7);
        if (MODE == 1 && p == 1 && wc == 1) acc[p] = MFMA(xf, wf, acc[p]);
        else acc[p] = MFMA(wf, xf, acc[p]);
      }
    }
  });

  const int row = row0 + xrow;
#pragma unroll
  for (int p = 0; p < NPW; ++p) {
    const int panel = wc * NPW + p;
    if (MODE == 1 && panel >= 2) {
      const int tile = (row0 >> 5) + wr; u16* kv = out1 + (size_t)tile * 2048;
      if (panel == 2) {
        float ss = 0.f;
#pragma unroll
        for (int g = 0; g < 4; ++g) { u32x2 v;
          if (g == 3 && hi == 1) v = u32x2{0x3F803F80u, 0u};
          else { v = u32x2{cvtpk(acc[p][4 * g], acc[p][4 * g + 1]), cvtpk(acc[p][4 * g + 2], acc[p][4 * g + 3])};
#pragma unroll
            for (int i = 0; i < 2; ++i) { const float a = __uint_as_float(v[i] << 16), b = __uint_as_float(v[i] & 0xffff0000u); ss = __builtin_fmaf(a, a, ss); ss = __builtin_fmaf(b, b, ss); } }
          *(u32x2*)(kv + (g >> 1) * 512 + (r32 + 32 * (g & 1)) * 8 + 4 * hi) = v; }
        ss += __shfl_xor(ss, 32, 64);
#pragma unroll
        for (int o = 16; o > 0; o >>= 1) ss = __builtin_fmaxf(ss, __shfl_xor(ss, o, 64));
        if (lane == 0) ((float*)((char*)out1 + (WS_KN - WS_KV)))[tile] = ss;
      } else {
#pragma unroll
        for (int g = 0; g < 4; ++g) { u32x2 v = u32x2{cvtpk(acc[p][4 * g], acc[p][4 * g + 1]), cvtpk(acc[p][4 * g + 2], acc[p][4 * g + 3])};
          if (r32 == 28) v = u32x2{0x3F803F80u, 0x3F803F80u}; else if (r32 > 28) v = u32x2{0u, 0u};
          *(u32x2*)(kv + 1024 + (g >> 1) * 512 + lane * 8 + 4 * (g & 1)) = v; }
      }
    } else if (MODE == 0 || MODE == 1) {
#pragma unroll
      for (int g = 0; g < 4; ++g) { const int c = col0 + panel * 32 + 8 * g + 4 * hi; const int head = c / 28, d = c - head * 28;
        u32x2 v = u32x2{cvtpk(acc[p][4 * g] * QSCALE, acc[p][4 * g + 1] * QSCALE), cvtpk(acc[p][4 * g + 2] * QSCALE, acc[p][4 * g + 3] * QSCALE)};
        u16* dst = out0 + ((size_t)head * S + row) * 32 + d;
        if (d == 24) *(u32x4*)dst = u32x4{v[0], v[1], 0u, 0u}; else *(u32x2*)dst = v; }
    } else if (MODE == 2) {
      if (col0 + panel * 32 < 1344) {
#pragma unroll
        for (int g = 0; g < 4; ++g) { const int c = col0 + panel * 32 + 8 * g + 4 * hi;
          *(u32x2*)(out0 + (size_t)row * 1344 + c) = u32x2{cvtpk(acc[p][4 * g], acc[p][4 * g + 1]), cvtpk(acc[p][4 * g + 2], acc[p][4 * g + 3])}; }
      }
    } else {
#pragma unroll
      for (int g = 0; g < 4; ++g) { const int c = col0 + panel * 32 + 8 * g + 4 * hi;
        *(u32x2*)(out0 + (size_t)row * 896 + c) = u32x2{cvtpk(acc[p][4 * g], acc[p][4 * g + 1]), cvtpk(acc[p][4 * g + 2], acc[p][4 * g + 3])}; }
    }
  }
}

__global__ __launch_bounds__(512, 2) void k1_proj(const u16* __restrict__ Ax, const u16* __restrict__ Awq, const u16* __restrict__ Awk, const u16* __restrict__ Awv,
                                                  const u16* __restrict__ Bx, const u16* __restrict__ Bw, char* __restrict__ ws) {
  extern __shared__ __attribute__((aligned(16))) char lds[];
  const int b = blockIdx.x, xcd = b & 7, j = b >> 3; const int rt = 2 * xcd + (j & 1), ct = j >> 1; const int row0 = rt * 128;
  u16* Qs = (u16*)(ws + WS_Q); u16* KV = (u16*)(ws + WS_KV); u16* QKV = (u16*)(ws + WS_QKV);
  if (ct < 3) gemm_tile<0, 4>(lds, Ax, DM, row0, Awq, DM, ct * 128, DM, nullptr, nullptr, Qs, nullptr);
  else if (ct == 3) gemm_tile<1, 4>(lds, Ax, DM, row0, Awq, DM, 384, DM, Awk, Awv, Qs, KV);
  else gemm_tile<2, 4>(lds, Bx, DM, row0, Bw, 1344, (ct - 4) * 128, 1344, nullptr, nullptr, QKV, nullptr);
}

__global__ __launch_bounds__(512, 2) void k3_out(const char* __restrict__ ws, const u16* __restrict__ Awo, const u16* __restrict__ Bwo, u16* __restrict__ out) {
  extern __shared__ __attribute__((aligned(16))) char lds[];
  const int b = blockIdx.x, xcd = b & 7, j = b >> 3; const int rt = 2 * xcd + (j & 1), ct = j >> 1; const int row0 = rt * 128;
  const u16* O = (const u16*)(ws + WS_O);
  const int part = ct >= 7 ? 1 : 0; const int c0 = (ct - 7 * part) * 64;
  gemm_tile<3, 2>(lds, O + part * 448, 896, row0, part ? Bwo : Awo, DM, c0, DM, nullptr, nullptr, out + part * 448, nullptr);
}

constexpr int A_STAGE = 16384;
constexpr int A_LDS_SIDE = 4 * A_STAGE;
constexpr int A_LDS_MRG = A_LDS_SIDE + 65536;
constexpr int A_LDS = A_LDS_MRG + 4 * 17 * 256;
constexpr float THR = 30.f;

DI float max16(const f32x16& s) {
  float a = __builtin_fmaxf(__builtin_fmaxf(s[0], s[1]), s[2]), b = __builtin_fmaxf(__builtin_fmaxf(s[3], s[4]), s[5]);
  a = __builtin_fmaxf(__builtin_fmaxf(a, s[6]), s[7]); b = __builtin_fmaxf(__builtin_fmaxf(b, s[8]), s[9]);
  a = __builtin_fmaxf(__builtin_fmaxf(a, s[10]), s[11]); b = __builtin_fmaxf(__builtin_fmaxf(b, s[12]), s[13]);
  a = __builtin_fmaxf(__builtin_fmaxf(a, s[14]), s[15]);
  return __builtin_fmaxf(a, b); }
DI float xhalf_max(float m) { auto rr = __builtin_amdgcn_permlane32_swap(__float_as_uint(m), __float_as_uint(m), false, false);
  return __builtin_fmaxf(__uint_as_float(rr[0]), __uint_as_float(rr[1])); }

DI float bf_round(float x) { return __uint_as_float(cvtpk(x, 0.f) << 16); }
#define SB() __builtin_amdgcn_sched_barrier(0)

__global__ __launch_bounds__(512, 2) void k2_attn(char* __restrict__ ws) {
  extern __shared__ __attribute__((aligned(16))) char lds[];
  const int tid = threadIdx.x, lane = tid & 63, r32 = lane & 31, hi = lane >> 5;
  const int wid = (int)sfl(tid >> 6), qg = wid & 3, kh = wid >> 2;
  const int b = blockIdx.x, xcd = b & 7, j = b >> 3;
  const int rc = xcd * 8 + (j >> 2), hg = j & 3;
  const int head = hg * 4 + qg, row0 = rc * 32;
  const u16* Qs = (const u16*)(ws + WS_Q); const char* KV = ws + WS_KV; const char* QKV = ws + WS_QKV; u16* O = (u16*)(ws + WS_O);
  const unsigned lds0 = (unsigned)(uintptr_t)lds;

  const u16* qp = Qs + ((size_t)head * S + row0 + r32) * 32 + hi * 8;
  const bf16x8 q0 = *(const bf16x8*)qp; u32x4 q1 = *(const u32x4*)(qp + 16);
  float kn2 = ((const float*)(ws + WS_KN))[lane];

  const char* kvsrc = KV + (size_t)(kh * 32) * 4096 + (2 * qg) * 1024 + lane * 16;
  const unsigned kvdst = kh * 8192 + (2 * qg) * 1024;
  auto issue_kv = [&](int s, int buf) { const unsigned d = sfl(lds0 + buf * A_STAGE + kvdst);
    glds16(kvsrc + (size_t)s * 8192, d); glds16(kvsrc + (size_t)s * 8192 + 1024, d + 1024); };
  const int brow0 = (xcd * 32 + j) * 8; const int bstart = brow0 >= 14 ? brow0 - 14 : 0;
  const char* sdsrc = QKV + (size_t)bstart * 2688 + wid * 1024 + lane * 16;
  auto issue_side = [&](int s) { glds16(sdsrc + s * 8192, sfl(lds0 + A_LDS_SIDE + s * 8192 + wid * 1024)); };

  issue_kv(0, 0); issue_kv(1, 1);

  float qn2 = 0.f;
  { const u32x4 a = __builtin_bit_cast(u32x4, q0);
#pragma unroll
    for (int i = 0; i < 4; ++i) { float x = __uint_as_float(a[i] << 16), y = __uint_as_float(a[i] & 0xffff0000u); qn2 = __builtin_fmaf(x, x, qn2); qn2 = __builtin_fmaf(y, y, qn2);
      x = __uint_as_float(q1[i] << 16); y = __uint_as_float(q1[i] & 0xffff0000u); qn2 = __builtin_fmaf(x, x, qn2); qn2 = __builtin_fmaf(y, y, qn2); } }
  qn2 += __shfl_xor(qn2, 32, 64);
#pragma unroll
  for (int o = 32; o > 0; o >>= 1) kn2 = __builtin_fmaxf(kn2, __shfl_xor(kn2, o, 64));
  const float bound = __builtin_sqrtf(qn2 * kn2) * 1.01f + 1.0f;

  f32x16 o = f32x16{};
  const lds_cptr rd0 = (lds_cptr)lds + kh * 8192 + lane * 16;
  float mref;
  auto set_ref = [&](float m) {
    const float mh = bf_round(m), ml = bf_round(m - mh); mref = mh + ml;
    const unsigned pk = cvtpk(-mh, -ml); if (hi) q1[2] = pk; };
#define Q1 __builtin_bit_cast(bf16x8, q1)

  wait_bar<2>();
  issue_side(0); issue_kv(2, 2);
  { const bf16x8 kf0 = lds_rd128(rd0), kf1 = lds_rd128(rd0 + 1024);
    f32x16 s = MFMA(kf0, q0, f32x16{}); s = MFMA(kf1, Q1, s);
    set_ref(xhalf_max(max16(s))); }
  const bool fast = __all(bound - mref <= 96.f);

  if (fast) {
    f32x16 sc, sn;
    { const bf16x8 kf0 = lds_rd128(rd0), kf1 = lds_rd128(rd0 + 1024); sc = MFMA(kf0, q0, f32x16{}); sc = MFMA(kf1, Q1, sc); }
#define EXPK(S_, i) cvtpk(__builtin_amdgcn_exp2f(S_[i]), __builtin_amdgcn_exp2f(S_[i + 1]))
#define ITER(SC_, SN_, PK_, PV_, HASN) do { \
      bf16x8 kf0, kf1; if (HASN) { kf0 = lds_rd128(PK_); kf1 = lds_rd128((PK_) + 1024); } \
      const bf16x8 vf0 = lds_rd128((PV_) + 2048), vf1 = lds_rd128((PV_) + 3072); SB(); \
      u32x4 p0, p1; \
      p0[0] = EXPK(SC_, 0); p0[1] = EXPK(SC_, 2); p0[2] = EXPK(SC_, 4); p0[3] = EXPK(SC_, 6); SB(); \
      if (HASN) SN_ = MFMA(kf0, q0, f32x16{}); SB(); \
      p1[0] = EXPK(SC_, 8); p1[1] = EXPK(SC_, 10); SB(); \
      o = MFMA(vf0, __builtin_bit_cast(bf16x8, p0), o); SB(); \
      p1[2] = EXPK(SC_, 12); SB(); \
      if (HASN) SN_ = MFMA(kf1, Q1, SN_); SB(); \
      p1[3] = EXPK(SC_, 14); SB(); \
      o = MFMA(vf1, __builtin_bit_cast(bf16x8, p1), o); SB(); } while (0)
    for (int s = 0; s < 15; ++s) {
      const lds_cptr p = rd0 + (s & 3) * A_STAGE, pn = rd0 + ((s + 1) & 3) * A_STAGE;
      ITER(sc, sn, p + 4096, p, true);
      if (s < 13) { wait_bar<2>(); if (s < 7) issue_side(s + 1); issue_kv(s + 3, (s + 3) & 3); } else wait_bar<0>();
      ITER(sn, sc, pn, p + 4096, true);
    }
    { const lds_cptr p = rd0 + 3 * A_STAGE; ITER(sc, sn, p + 4096, p, true); ITER(sn, sc, p, p + 4096, false); }
  } else {
    auto tile = [&](lds_cptr p) {
      const bf16x8 kf0 = lds_rd128(p), kf1 = lds_rd128(p + 1024), vf0 = lds_rd128(p + 2048), vf1 = lds_rd128(p + 3072);
      f32x16 s = MFMA(kf0, q0, f32x16{}); s = MFMA(kf1, Q1, s);
      const float tm = max16(s);
      if (__any(tm > THR)) {
        const float old = mref; set_ref(mref + __builtin_fmaxf(xhalf_max(tm), 0.f)); const float dl = mref - old; const float al = __builtin_amdgcn_exp2f(-dl);
#pragma unroll
        for (int r = 0; r < 16; ++r) { s[r] -= dl; o[r] *= al; }
      }
      u32x4 p0, p1;
#pragma unroll
      for (int i = 0; i < 4; ++i) { p0[i] = EXPK(s, 2 * i); p1[i] = EXPK(s, 8 + 2 * i); }
      o = MFMA(vf0, __builtin_bit_cast(bf16x8, p0), o); o = MFMA(vf1, __builtin_bit_cast(bf16x8, p1), o);
    };
    for (int s = 0; s < 15; ++s) {
      const lds_cptr p = rd0 + (s & 3) * A_STAGE;
      tile(p);
      if (s < 13) { wait_bar<2>(); if (s < 7) issue_side(s + 1); issue_kv(s + 3, (s + 3) & 3); } else wait_bar<0>();
      tile(p + 4096);
    }
    { const lds_cptr p = rd0 + 3 * A_STAGE; tile(p); tile(p + 4096); }
  }

  float* mrg = (float*)(lds + A_LDS_MRG) + qg * 17 * 64;
  if (kh == 1) {
#pragma unroll
    for (int r = 0; r < 16; ++r) mrg[r * 64 + lane] = o[r];
    mrg[16 * 64 + lane] = -mref;
  }
  __syncthreads();
  if (kh == 0) {
    const float nb = mrg[16 * 64 + lane], na = -mref;
    const float nm = __builtin_fminf(na, nb);
    const float fa = __builtin_amdgcn_exp2f(nm - na), fb = __builtin_amdgcn_exp2f(nm - nb);
#pragma unroll
    for (int r = 0; r < 16; ++r) o[r] = o[r] * fa + mrg[r * 64 + lane] * fb;
    const float l = __shfl(o[12], r32 + 32, 64);
    const float il = 1.0f / l;
    u16* dst = O + (size_t)(row0 + r32) * 896 + head * 28 + 4 * hi;
#pragma unroll
    for (int g = 0; g < 4; ++g) if (!(g == 3 && hi == 1))
      *(u32x2*)(dst + 8 * g) = u32x2{cvtpk(o[4 * g] * il, o[4 * g + 1] * il), cvtpk(o[4 * g + 2] * il, o[4 * g + 3] * il)};
  } else {
    const int t = tid - 256, pr = t >> 1, hf = t & 1, rl = pr >> 4, hb = pr & 15; const int r = brow0 + rl;
    const char* img = lds + A_LDS_SIDE + hb * 56 + hf * 28;
    const unsigned* qv = (const unsigned*)(img + (r - bstart) * 2688);
    float qf[14];
#pragma unroll
    for (int i = 0; i < 7; ++i) { const unsigned u = qv[i]; qf[2 * i] = __uint_as_float(u << 16); qf[2 * i + 1] = __uint_as_float(u & 0xffff0000u); }
    float lg[8]; float m = -3.0e38f;
#pragma unroll
    for (int w = 0; w < 8; ++w) { const int jr = r - 2 * w; const int jc = jr >= 0 ? jr : bstart;
      const unsigned* kp = (const unsigned*)(img + (jc - bstart) * 2688 + 896); float a = 0.f;
#pragma unroll
      for (int i = 0; i < 7; ++i) { const unsigned u = kp[i]; a = __builtin_fmaf(qf[2 * i], __uint_as_float(u << 16), a); a = __builtin_fmaf(qf[2 * i + 1], __uint_as_float(u & 0xffff0000u), a); }
      a += __shfl_xor(a, 1, 64);
      lg[w] = jr >= 0 ? a * SCALE : -1e9f; m = __builtin_fmaxf(m, lg[w]); }
    float l = 0.f;
#pragma unroll
    for (int w = 0; w < 8; ++w) { lg[w] = __builtin_amdgcn_exp2f((lg[w] - m) * 1.4426950408889634f); l += lg[w]; }
    const float il = 1.0f / l; float oa[14];
#pragma unroll
    for (int i = 0; i < 14; ++i) oa[i] = 0.f;
#pragma unroll
    for (int w = 0; w < 8; ++w) { const int jr = r - 2 * w; const int jc = jr >= 0 ? jr : bstart; const float pw = lg[w] * il;
      const unsigned* vp = (const unsigned*)(img + (jc - bstart) * 2688 + 1792);
#pragma unroll
      for (int i = 0; i < 7; ++i) { const unsigned u = vp[i]; oa[2 * i] = __builtin_fmaf(pw, __uint_as_float(u << 16), oa[2 * i]); oa[2 * i + 1] = __builtin_fmaf(pw, __uint_as_float(u & 0xffff0000u), oa[2 * i + 1]); } }
    unsigned* dst = (unsigned*)(O + (size_t)r * 896 + 448 + hb * 28 + hf * 14);
#pragma unroll
    for (int i = 0; i < 7; ++i) dst[i] = cvtpk(oa[2 * i], oa[2 * i + 1]);
  }
}

extern "C" void kernel_launch(void* const* d_in, const int* in_sizes, int n_in, void* d_out, int out_size, void* d_ws, size_t ws_size, hipStream_t stream) {
  const u16 *Ax = (const u16*)d_in[0], *Awq = (const u16*)d_in[1], *Awk = (const u16*)d_in[2], *Awv = (const u16*)d_in[3], *Awo = (const u16*)d_in[4];
  const u16 *Bx = (const u16*)d_in[5], *Bw = (const u16*)d_in[6], *Bwo = (const u16*)d_in[7];
  char* ws = (char*)d_ws;
  (void)hipFuncSetAttribute((const void*)k1_proj, hipFuncAttributeMaxDynamicSharedMemorySize, GemmCfg<4>::LDS);
  (void)hipFuncSetAttribute((const void*)k2_attn, hipFuncAttributeMaxDynamicSharedMemorySize, A_LDS);
  (void)hipFuncSetAttribute((const void*)k3_out, hipFuncAttributeMaxDynamicSharedMemorySize, GemmCfg<2>::LDS);
  k1_proj<<<240, 512, GemmCfg<4>::LDS, stream>>>(Ax, Awq, Awk, Awv, Bx, Bw, ws);
  k2_attn<<<256, 512, A_LDS, stream>>>(ws);
  k3_out<<<224, 512, GemmCfg<2>::LDS, stream>>>(ws, Awo, Bwo, (u16*)d_out);
}
```

```cpp
#include <hip/hip_runtime.h>
#include <stdint.h>

typedef unsigned short u16;
using bf16x8 = __attribute__((ext_vector_type(8))) short;
using s16x4  = __attribute__((ext_vector_type(4))) short;
using f32x16 = __attribute__((ext_vector_type(16))) float;
using u32x4  = __attribute__((ext_vector_type(4))) unsigned;
using u32x2  = __attribute__((ext_vector_type(2))) unsigned;
typedef __attribute__((address_space(3))) const char* lds_cptr;
typedef short v4i16_t __attribute__((ext_vector_type(4)));
#define DI __device__ __forceinline__
#define MFMA(a, b, c) __builtin_amdgcn_mfma_f32_32x32x16_bf16((a), (b), (c), 0, 0, 0)

constexpr int S = 2048, DM = 448, NH = 16, DH = 28;
constexpr float SCALE = 0.18898223650461362f;
constexpr float QSCALE = SCALE * 1.4426950408889634f;

constexpr size_t WS_Q   = 0;
constexpr size_t WS_KV  = WS_Q + (size_t)NH * S * 32 * 2;
constexpr size_t WS_KN  = WS_KV + (size_t)64 * 4096;
constexpr size_t WS_QKV = WS_KN + 256;
constexpr size_t WS_O   = WS_QKV + (size_t)S * 1344 * 2 + 4096;

typedef __bf16 bf16x2_t __attribute__((ext_vector_type(2)));
typedef float f32x2_t __attribute__((ext_vector_type(2)));
DI unsigned cvtpk(float lo, float hi) { f32x2_t v = {lo, hi}; return __builtin_bit_cast(unsigned, __builtin_convertvector(v, bf16x2_t)); }
DI void glds16(const void* g, unsigned lds_base) {
  unsigned sv; asm volatile("s_mov_b32 %0, m0\n\ts_mov_b32 m0, %2\n\ts_nop 0\n\tglobal_load_lds_dwordx4 %1, off\n\ts_mov_b32 m0, %0"
                            : "=&s"(sv) : "v"(g), "s"(lds_base) : "memory"); }
DI void glds4(const void* g, unsigned lds_base) {
  unsigned sv; asm volatile("s_mov_b32 %0, m0\n\ts_mov_b32 m0, %2\n\ts_nop 0\n\tglobal_load_lds_dword %1, off\n\ts_mov_b32 m0, %0"
                            : "=&s"(sv) : "v"(g), "s"(lds_base) : "memory"); }
template <int N> DI void wait_bar() { asm volatile("s_waitcnt vmcnt(%0) lgkmcnt(0)\n\ts_barrier" :: "i"(N) : "memory"); }
DI bf16x8 lds_rd128(lds_cptr p) { return *(const __attribute__((address_space(3))) bf16x8*)p; }
DI s16x4 lds_tr(lds_cptr p) { return __builtin_bit_cast(s16x4, __builtin_amdgcn_ds_read_tr16_b64_v4i16((__attribute__((address_space(3))) v4i16_t*)p)); }
DI unsigned sfl(unsigned x) { return (unsigned)__builtin_amdgcn_readfirstlane((int)x); }
template <int I> struct IC { static constexpr int value = I; };
template <int I, int N, class F> DI void static_for(F&& f) { if constexpr (I < N) { f(IC<I>{}); static_for<I + 1, N>(f); } }

constexpr int G_NBUF = 4;
template <int NP> struct GemmCfg { static constexpr int STAGE_BYTES = 16384 + NP * 4096; static constexpr int LDS = STAGE_BYTES * G_NBUF; };

struct GemmArgs {
  const u16* X; const u16* W; const u16* Wk; const u16* Wv; u16* out0; u16* out1;
};

template <int MODE, int NP>
DI void gemm_tile(char* lds, const u16* __restrict__ X, int ldx, int row0, const u16* __restrict__ W, int ldw, int col0, int ncols,
                  const u16* __restrict__ Wk, const u16* __restrict__ Wv, u16* __restrict__ out0, u16* __restrict__ out1) {
  constexpr int SB = GemmCfg<NP>::STAGE_BYTES;
  constexpr int NPW = NP / 2;
  constexpr int L = (MODE == 1) ? 7 : (2 + NP / 2);
  const int tid = threadIdx.x, lane = tid & 63, r32 = lane & 31, hi = lane >> 5;
  const int wid = (int)sfl(tid >> 6);
  const unsigned lds0 = (unsigned)(uintptr_t)lds;
  const u16* xsrc[2];
#pragma unroll
  for (int i = 0; i < 2; ++i) { const int row = (2 * wid + i) * 8 + (lane >> 3); const int c = (lane & 7) ^ ((row >> 1) & 7);
    xsrc[i] = X + (size_t)(row0 + row) * ldx + c * 8; }
  const u16* wsrc[2]; unsigned wdst[2];
  if constexpr (MODE == 1) {
    const int panel = wid >> 2, piece = wid & 3;
    wsrc[0] = W + (size_t)(piece * 16 + (lane >> 2)) * ldw + col0 + panel * 32 + (lane & 3) * 8;
    wdst[0] = 16384 + panel * 4096 + piece * 1024;
    wsrc[1] = nullptr; wdst[1] = 0;
  } else if constexpr (NP == 4) {
    const int panel = wid >> 1; int pc = col0 + panel * 32; if (pc > ncols - 32) pc = ncols - 32;
#pragma unroll
    for (int i = 0; i < 2; ++i) { const int piece = 2 * (wid & 1) + i;
      wsrc[i] = W + (size_t)(piece * 16 + (lane >> 2)) * ldw + pc + (lane & 3) * 8; wdst[i] = 16384 + panel * 4096 + piece * 1024; }
  } else {
    const int panel = wid >> 2, piece = wid & 3;
    wsrc[0] = W + (size_t)(piece * 16 + (lane >> 2)) * ldw + col0 + panel * 32 + (lane & 3) * 8;
    wdst[0] = 16384 + panel * 4096 + piece * 1024;
    wsrc[1] = nullptr; wdst[1] = 0;
  }
  const int kvoff = (lane >> 4) * 28 + ((lane & 15) < 14 ? (lane & 15) : 13) * 2;

  auto issue = [&](int s) {
    const unsigned base = lds0 + (unsigned)((s % G_NBUF) * SB);
    glds16(xsrc[0] + s * 64, sfl(base + (2 * wid) * 1024));
    glds16(xsrc[1] + s * 64, sfl(base + (2 * wid + 1) * 1024));
    glds16(wsrc[0] + (size_t)s * 64 * ldw, sfl(base + wdst[0]));
    if constexpr (MODE != 1 && NP == 4) glds16(wsrc[1] + (size_t)s * 64 * ldw, sfl(base + wdst[1]));
    if constexpr (MODE == 1) {
#pragma unroll
      for (int i = 0; i < 2; ++i) { const int piece = 2 * wid + i;
        glds4(Wk + (s * 64 + piece * 4) * 28 + kvoff, sfl(base + 16384 + 2 * 4096 + piece * 256));
        glds4(Wv + (s * 64 + piece * 4) * 28 + kvoff, sfl(base + 16384 + 3 * 4096 + piece * 256)); }
    }
  };

  const int wr = wid & 3, wc = wid >> 2;
  const int xrow = wr * 32 + r32; const int xsw = (xrow >> 1) & 7;
  const int g16 = lane >> 4, i16 = lane & 15;
  const int woff = 16384 + (wc * NPW) * 4096 + (8 * (g16 >> 1) + (i16 >> 2)) * 64 + (g16 & 1) * 32 + (i16 & 3) * 8;

  f32x16 acc[NPW];
#pragma unroll
  for (int p = 0; p < NPW; ++p) acc[p] = f32x16{};

  issue(0); issue(1); issue(2);
  static_for<0, 7>([&](auto sc) {
    constexpr int s = decltype(sc)::value;
    constexpr int ahead = (s + 2 < 6 ? s + 2 : 6) - s;
    wait_bar<ahead * L>();
    if constexpr (s + 3 <= 6) issue(s + 3);
    const lds_cptr b = (lds_cptr)lds + (s % G_NBUF) * SB;
#pragma unroll
    for (int ks = 0; ks < 4; ++ks) {
      const bf16x8 xf = lds_rd128(b + xrow * 128 + (((2 * ks + hi) ^ xsw) * 16));
#pragma unroll
      for (int p = 0; p < NPW; ++p) {
        const s16x4 lo = lds_tr(b + woff + p * 4096 + ks * 1024), hh = lds_tr(b + woff + p * 4096 + ks * 1024 + 256);
        const bf16x8 wf = __builtin_shufflevector(lo, hh, 0, 1, 2, 3, 4, 5, 6, 7);
        if (MODE == 1 && p == 1 && wc == 1) acc[p] = MFMA(xf, wf, acc[p]);
        else acc[p] = MFMA(wf, xf, acc[p]);
      }
    }
  });

  const int row = row0 + xrow;
#pragma unroll
  for (int p = 0; p < NPW; ++p) {
    const int panel = wc * NPW + p;
    if (MODE == 1 && panel >= 2) {
      const int tile = (row0 >> 5) + wr; u16* kv = out1 + (size_t)tile * 2048;
      if (panel == 2) {
        float ss = 0.f;
#pragma unroll
        for (int g = 0; g < 4; ++g) { u32x2 v;
          if (g == 3 && hi == 1) v = u32x2{0x3F803F80u, 0u};
          else { v = u32x2{cvtpk(acc[p][4 * g], acc[p][4 * g + 1]), cvtpk(acc[p][4 * g + 2], acc[p][4 * g + 3])};
#pragma unroll
            for (int i = 0; i < 2; ++i) { const float a = __uint_as_float(v[i] << 16), b = __uint_as_float(v[i] & 0xffff0000u); ss = __builtin_fmaf(a, a, ss); ss = __builtin_fmaf(b, b, ss); } }
          *(u32x2*)(kv + (g >> 1) * 512 + (r32 + 32 * (g & 1)) * 8 + 4 * hi) = v; }
        ss += __shfl_xor(ss, 32, 64);
#pragma unroll
        for (int o = 16; o > 0; o >>= 1) ss = __builtin_fmaxf(ss, __shfl_xor(ss, o, 64));
        if (lane == 0) ((float*)((char*)out1 + (WS_KN - WS_KV)))[tile] = ss;
      } else {
#pragma unroll
        for (int g = 0; g < 4; ++g) { u32x2 v = u32x2{cvtpk(acc[p][4 * g], acc[p][4 * g + 1]), cvtpk(acc[p][4 * g + 2], acc[p][4 * g + 3])};
          if (r32 == 28) v = u32x2{0x3F803F80u, 0x3F803F80u}; else if (r32 > 28) v = u32x2{0u, 0u};
          *(u32x2*)(kv + 1024 + (g >> 1) * 512 + lane * 8 + 4 * (g & 1)) = v; }
      }
    } else if (MODE == 0 || MODE == 1) {
#pragma unroll
      for (int g = 0; g < 4; ++g) { const int c = col0 + panel * 32 + 8 * g + 4 * hi; const int head = c / 28, d = c - head * 28;
        u32x2 v = u32x2{cvtpk(acc[p][4 * g] * QSCALE, acc[p][4 * g + 1] * QSCALE), cvtpk(acc[p][4 * g + 2] * QSCALE, acc[p][4 * g + 3] * QSCALE)};
        u16* dst = out0 + ((size_t)head * S + row) * 32 + d;
        if (d == 24) *(u32x4*)dst = u32x4{v[0], v[1], 0u, 0u}; else *(u32x2*)dst = v; }
    } else if (MODE == 2) {
      if (col0 + panel * 32 < 1344) {
#pragma unroll
        for (int g = 0; g < 4; ++g) { const int c = col0 + panel * 32 + 8 * g + 4 * hi;
          *(u32x2*)(out0 + (size_t)row * 1344 + c) = u32x2{cvtpk(acc[p][4 * g], acc[p][4 * g + 1]), cvtpk(acc[p][4 * g + 2], acc[p][4 * g + 3])}; }
      }
    } else {
#pragma unroll
      for (int g = 0; g < 4; ++g) { const int c = col0 + panel * 32 + 8 * g + 4 * hi;
        *(u32x2*)(out0 + (size_t)row * 896 + c) = u32x2{cvtpk(acc[p][4 * g], acc[p][4 * g + 1]), cvtpk(acc[p][4 * g + 2], acc[p][4 * g + 3])}; }
    }
  }
}

__global__ __launch_bounds__(512, 2) void k1_proj(const u16* __restrict__ Ax, const u16* __restrict__ Awq, const u16* __restrict__ Awk, const u16* __restrict__ Awv,
                                                  const u16* __restrict__ Bx, const u16* __restrict__ Bw, char* __restrict__ ws) {
  extern __shared__ __attribute__((aligned(16))) char lds[];
  const int b = blockIdx.x, xcd = b & 7, j = b >> 3; const int rt = 2 * xcd + (j & 1), ct = j >> 1; const int row0 = rt * 128;
  u16* Qs = (u16*)(ws + WS_Q); u16* KV = (u16*)(ws + WS_KV); u16* QKV = (u16*)(ws + WS_QKV);
  if (ct < 3) gemm_tile<0, 4>(lds, Ax, DM, row0, Awq, DM, ct * 128, DM, nullptr, nullptr, Qs, nullptr);
  else if (ct == 3) gemm_tile<1, 4>(lds, Ax, DM, row0, Awq, DM, 384, DM, Awk, Awv, Qs, KV);
  else gemm_tile<2, 4>(lds, Bx, DM, row0, Bw, 1344, (ct - 4) * 128, 1344, nullptr, nullptr, QKV, nullptr);
}

__global__ __launch_bounds__(512, 2) void k3_out(const char* __restrict__ ws, const u16* __restrict__ Awo, const u16* __restrict__ Bwo, u16* __restrict__ out) {
  extern __shared__ __attribute__((aligned(16))) char lds[];
  const int b = blockIdx.x, xcd = b & 7, j = b >> 3; const int rt = 2 * xcd + (j & 1), ct = j >> 1; const int row0 = rt * 128;
  const u16* O = (const u16*)(ws + WS_O);
  const int part = ct >= 7 ? 1 : 0; const int c0 = (ct - 7 * part) * 64;
  gemm_tile<3, 2>(lds, O + part * 448, 896, row0, part ? Bwo : Awo, DM, c0, DM, nullptr, nullptr, out + part * 448, nullptr);
}

constexpr int A_STAGE = 16384;
constexpr int A_LDS_SIDE = 4 * A_STAGE;
constexpr int A_LDS_MRG = A_LDS_SIDE + 65536;
constexpr int A_LDS = A_LDS_MRG + 4 * 17 * 256;
constexpr float THR = 30.f;

DI float max16(const f32x16& s) {
  float a = __builtin_fmaxf(__builtin_fmaxf(s[0], s[1]), s[2]), b = __builtin_fmaxf(__builtin_fmaxf(s[3], s[4]), s[5]);
  a = __builtin_fmaxf(__builtin_fmaxf(a, s[6]), s[7]); b = __builtin_fmaxf(__builtin_fmaxf(b, s[8]), s[9]);
  a = __builtin_fmaxf(__builtin_fmaxf(a, s[10]), s[11]); b = __builtin_fmaxf(__builtin_fmaxf(b, s[12]), s[13]);
  a = __builtin_fmaxf(__builtin_fmaxf(a, s[14]), s[15]);
  return __builtin_fmaxf(a, b); }
DI float xhalf_max(float m) { auto rr = __builtin_amdgcn_permlane32_swap(__float_as_uint(m), __float_as_uint(m), false, false);
  return __builtin_fmaxf(__uint_as_float(rr[0]), __uint_as_float(rr[1])); }

DI float bf_round(float x) { return __uint_as_float(cvtpk(x, 0.f) << 16); }
#define SB() __builtin_amdgcn_sched_barrier(0)

__global__ __launch_bounds__(512, 2) void k2_attn(char* __restrict__ ws) {
  extern __shared__ __attribute__((aligned(16))) char lds[];
  const int tid = threadIdx.x, lane = tid & 63, r32 = lane & 31, hi = lane >> 5;
  const int wid = (int)sfl(tid >> 6), qg = wid & 3, kh = wid >> 2;
  const int b = blockIdx.x, xcd = b & 7, j = b >> 3;
  const int rc = xcd * 8 + (j >> 2), hg = j & 3;
  const int head = hg * 4 + qg, row0 = rc * 32;
  const u16* Qs = (const u16*)(ws + WS_Q); const char* KV = ws + WS_KV; const char* QKV = ws + WS_QKV; u16* O = (u16*)(ws + WS_O);
  const unsigned lds0 = (unsigned)(uintptr_t)lds;

  const u16* qp = Qs + ((size_t)head * S + row0 + r32) * 32 + hi * 8;
  const bf16x8 q0 = *(const bf16x8*)qp; u32x4 q1 = *(const u32x4*)(qp + 16);
  float kn2 = ((const float*)(ws + WS_KN))[lane];

  const char* kvsrc = KV + (size_t)(kh * 32) * 4096 + (2 * qg) * 1024 + lane * 16;
  const unsigned kvdst = kh * 8192 + (2 * qg) * 1024;
  auto issue_kv = [&](int s, int buf) { const unsigned d = sfl(lds0 + buf * A_STAGE + kvdst);
    glds16(kvsrc + (size_t)s * 8192, d); glds16(kvsrc + (size_t)s * 8192 + 1024, d + 1024); };
  const int brow0 = (xcd * 32 + j) * 8; const int bstart = brow0 >= 14 ? brow0 - 14 : 0;
  const char* sdsrc = QKV + (size_t)bstart * 2688 + wid * 1024 + lane * 16;
  auto issue_side = [&](int s) { glds16(sdsrc + s * 8192, sfl(lds0 + A_LDS_SIDE + s * 8192 + wid * 1024)); };

  issue_kv(0, 0); issue_kv(1, 1);

  float qn2 = 0.f;
  { const u32x4 a = __builtin_bit_cast(u32x4, q0);
#pragma unroll
    for (int i = 0; i < 4; ++i) { float x = __uint_as_float(a[i] << 16), y = __uint_as_float(a[i] & 0xffff0000u); qn2 = __builtin_fmaf(x, x, qn2); qn2 = __builtin_fmaf(y, y, qn2);
      x = __uint_as_float(q1[i] << 16); y = __uint_as_float(q1[i] & 0xffff0000u); qn2 = __builtin_fmaf(x, x, qn2); qn2 = __builtin_fmaf(y, y, qn2); } }
  qn2 += __shfl_xor(qn2, 32, 64);
#pragma unroll
  for (int o = 32; o > 0; o >>= 1) kn2 = __builtin_fmaxf(kn2, __shfl_xor(kn2, o, 64));
  const float bound = __builtin_sqrtf(qn2 * kn2) * 1.01f + 1.0f;

  f32x16 o = f32x16{};
  const lds_cptr rd0 = (lds_cptr)lds + kh * 8192 + lane * 16;
  float mref, m0;
  auto set_ref = [&](float m) {
    const float mh = bf_round(m), ml = bf_round(m - mh); mref = mh + ml;
    const unsigned pk = cvtpk(-mh, -ml); if (hi) q1[2] = pk; };
#define Q1 __builtin_bit_cast(bf16x8, q1)

  wait_bar<2>();
  issue_side(0); issue_kv(2, 2);
  { const bf16x8 kf0 = lds_rd128(rd0), kf1 = lds_rd128(rd0 + 1024);
    f32x16 s = MFMA(kf0, q0, f32x16{}); s = MFMA(kf1, Q1, s);
    m0 = xhalf_max(max16(s)); }
  const bool fast = __all(bound - m0 <= 160.f);
  set_ref(fast ? __builtin_fmaxf(m0, bound - 100.f) : m0);

  if (fast) {
    f32x16 sc, sn;
    { const bf16x8 kf0 = lds_rd128(rd0), kf1 = lds_rd128(rd0 + 1024); sc = MFMA(kf0, q0, f32x16{}); sc = MFMA(kf1, Q1, sc); }
#define EXPK(S_, i) cvtpk(__builtin_amdgcn_exp2f(S_[i]), __builtin_amdgcn_exp2f(S_[i + 1]))
#define ITER(SC_, SN_, PK_, PV_, HASN) do { \
      bf16x8 kf0, kf1; if (HASN) { kf0 = lds_rd128(PK_); kf1 = lds_rd128((PK_) + 1024); } \
      const bf16x8 vf0 = lds_rd128((PV_) + 2048), vf1 = lds_rd128((PV_) + 3072); SB(); \
      u32x4 p0, p1; \
      p0[0] = EXPK(SC_, 0); p0[1] = EXPK(SC_, 2); p0[2] = EXPK(SC_, 4); p0[3] = EXPK(SC_, 6); SB(); \
      if (HASN) SN_ = MFMA(kf0, q0, f32x16{}); SB(); \
      p1[0] = EXPK(SC_, 8); p1[1] = EXPK(SC_, 10); SB(); \
      o = MFMA(vf0, __builtin_bit_cast(bf16x8, p0), o); SB(); \
      p1[2] = EXPK(SC_, 12); SB(); \
      if (HASN) SN_ = MFMA(kf1, Q1, SN_); SB(); \
      p1[3] = EXPK(SC_, 14); SB(); \
      o = MFMA(vf1, __builtin_bit_cast(bf16x8, p1), o); SB(); } while (0)
    for (int s = 0; s < 15; ++s) {
      const lds_cptr p = rd0 + (s & 3) * A_STAGE, pn = rd0 + ((s + 1) & 3) * A_STAGE;
      ITER(sc, sn, p + 4096, p, true);
      if (s < 13) { wait_bar<2>(); if (s < 7) issue_side(s + 1); issue_kv(s + 3, (s + 3) & 3); } else wait_bar<0>();
      ITER(sn, sc, pn, p + 4096, true);
    }
    { const lds_cptr p = rd0 + 3 * A_STAGE; ITER(sc, sn, p + 4096, p, true); ITER(sn, sc, p, p + 4096, false); }
  } else {
    auto tile = [&](lds_cptr p) {
      const bf16x8 kf0 = lds_rd128(p), kf1 = lds_rd128(p + 1024), vf0 = lds_rd128(p + 2048), vf1 = lds_rd128(p + 3072);
      f32x16 s = MFMA(kf0, q0, f32x16{}); s = MFMA(kf1, Q1, s);
      const float tm = max16(s);
      if (__any(tm > THR)) {
        const float old = mref; set_ref(mref + __builtin_fmaxf(xhalf_max(tm), 0.f)); const float dl = mref - old; const float al = __builtin_amdgcn_exp2f(-dl);
#pragma unroll
        for (int r = 0; r < 16; ++r) { s[r] -= dl; o[r] *= al; }
      }
      u32x4 p0, p1;
#pragma unroll
      for (int i = 0; i < 4; ++i) { p0[i] = EXPK(s, 2 * i); p1[i] = EXPK(s, 8 + 2 * i); }
      o = MFMA(vf0, __builtin_bit_cast(bf16x8, p0), o); o = MFMA(vf1, __builtin_bit_cast(bf16x8, p1), o);
    };
    for (int s = 0; s < 15; ++s) {
      const lds_cptr p = rd0 + (s & 3) * A_STAGE;
      tile(p);
      if (s < 13) { wait_bar<2>(); if (s < 7) issue_side(s + 1); issue_kv(s + 3, (s + 3) & 3); } else wait_bar<0>();
      tile(p + 4096);
    }
    { const lds_cptr p = rd0 + 3 * A_STAGE; tile(p); tile(p + 4096); }
  }

  float* mrg = (float*)(lds + A_LDS_MRG) + qg * 17 * 64;
  if (kh == 1) {
#pragma unroll
    for (int r = 0; r < 16; ++r) mrg[r * 64 + lane] = o[r];
    mrg[16 * 64 + lane] = -mref;
  }
  __syncthreads();
  if (kh == 0) {
    const float nb = mrg[16 * 64 + lane], na = -mref;
    const float nm = __builtin_fminf(na, nb);
    const float fa = __builtin_amdgcn_exp2f(nm - na), fb = __builtin_amdgcn_exp2f(nm - nb);
#pragma unroll
    for (int r = 0; r < 16; ++r) o[r] = o[r] * fa + mrg[r * 64 + lane] * fb;
    const float l = __shfl(o[12], r32 + 32, 64);
    const float il = 1.0f / l;
    u16* dst = O + (size_t)(row0 + r32) * 896 + head * 28 + 4 * hi;
#pragma unroll
    for (int g = 0; g < 4; ++g) if (!(g == 3 && hi == 1))
      *(u32x2*)(dst + 8 * g) = u32x2{cvtpk(o[4 * g] * il, o[4 * g + 1] * il), cvtpk(o[4 * g + 2] * il, o[4 * g + 3] * il)};
  } else {
    const int t = tid - 256, pr = t >> 1, hf = t & 1, rl = pr >> 4, hb = pr & 15; const int r = brow0 + rl;
    const char* img = lds + A_LDS_SIDE + hb * 56 + hf * 28;
    const unsigned* qv = (const unsigned*)(img + (r - bstart) * 2688);
    float qf[14];
#pragma unroll
    for (int i = 0; i < 7; ++i) { const unsigned u = qv[i]; qf[2 * i] = __uint_as_float(u << 16); qf[2 * i + 1] = __uint_as_float(u & 0xffff0000u); }
    float lg[8]; float m = -3.0e38f;
#pragma unroll
    for (int w = 0; w < 8; ++w) { const int jr = r - 2 * w; const int jc = jr >= 0 ? jr : bstart;
      const unsigned* kp = (const unsigned*)(img + (jc - bstart) * 2688 + 896); float a = 0.f;
#pragma unroll
      for (int i = 0; i < 7; ++i) { const unsigned u = kp[i]; a = __builtin_fmaf(qf[2 * i], __uint_as_float(u << 16), a); a = __builtin_fmaf(qf[2 * i + 1], __uint_as_float(u & 0xffff0000u), a); }
      a += __shfl_xor(a, 1, 64);
      lg[w] = jr >= 0 ? a * SCALE : -1e9f; m = __builtin_fmaxf(m, lg[w]); }
    float l = 0.f;
#pragma unroll
    for (int w = 0; w < 8; ++w) { lg[w] = __builtin_amdgcn_exp2f((lg[w] - m) * 1.4426950408889634f); l += lg[w]; }
    const float il = 1.0f / l; float oa[14];
#pragma unroll
    for (int i = 0; i < 14; ++i) oa[i] = 0.f;
#pragma unroll
    for (int w = 0; w < 8; ++w) { const int jr = r - 2 * w; const int jc = jr >= 0 ? jr : bstart; const float pw = lg[w] * il;
      const unsigned* vp = (const unsigned*)(img + (jc - bstart) * 2688 + 1792);
#pragma unroll
      for (int i = 0; i < 7; ++i) { const unsigned u = vp[i]; oa[2 * i] = __builtin_fmaf(pw, __uint_as_float(u << 16), oa[2 * i]); oa[2 * i + 1] = __builtin_fmaf(pw, __uint_as_float(u & 0xffff0000u), oa[2 * i + 1]); } }
    unsigned* dst = (unsigned*)(O + (size_t)r * 896 + 448 + hb * 28 + hf * 14);
#pragma unroll
    for (int i = 0; i < 7; ++i) dst[i] = cvtpk(oa[2 * i], oa[2 * i + 1]);
  }
}

extern "C" void kernel_launch(void* const* d_in, const int* in_sizes, int n_in, void* d_out, int out_size, void* d_ws, size_t ws_size, hipStream_t stream) {
  const u16 *Ax = (const u16*)d_in[0], *Awq = (const u16*)d_in[1], *Awk = (const u16*)d_in[2], *Awv = (const u16*)d_in[3], *Awo = (const u16*)d_in[4];
  const u16 *Bx = (const u16*)d_in[5], *Bw = (const u16*)d_in[6], *Bwo = (const u16*)d_in[7];
  char* ws = (char*)d_ws;
  (void)hipFuncSetAttribute((const void*)k1_proj, hipFuncAttributeMaxDynamicSharedMemorySize, GemmCfg<4>::LDS);
  (void)hipFuncSetAttribute((const void*)k2_attn, hipFuncAttributeMaxDynamicSharedMemorySize, A_LDS);
  (void)hipFuncSetAttribute((const void*)k3_out, hipFuncAttributeMaxDynamicSharedMemorySize, GemmCfg<2>::LDS);
  k1_proj<<<240, 512, GemmCfg<4>::LDS, stream>>>(Ax, Awq, Awk, Awv, Bx, Bw, ws);
  k2_attn<<<256, 512, A_LDS, stream>>>(ws);
  k3_out<<<224, 512, GemmCfg<2>::LDS, stream>>>(ws, Awo, Bwo, (u16*)d_out);
}
```

```cpp
#include <hip/hip_runtime.h>
#include <stdint.h>

typedef unsigned short u16;
using bf16x8 = __attribute__((ext_vector_type(8))) short;
using s16x4  = __attribute__((ext_vector_type(4))) short;
using f32x16 = __attribute__((ext_vector_type(16))) float;
using u32x4  = __attribute__((ext_vector_type(4))) unsigned;
using u32x2  = __attribute__((ext_vector_type(2))) unsigned;
typedef __attribute__((address_space(3))) const char* lds_cptr;
typedef __attribute__((address_space(3))) char* lds_ptr;
typedef short v4i16_t __attribute__((ext_vector_type(4)));
typedef __bf16 bf16x2_t __attribute__((ext_vector_type(2)));
typedef float f32x2_t __attribute__((ext_vector_type(2)));
#define DI __device__ __forceinline__
#define MFMA(a, b, c) __builtin_amdgcn_mfma_f32_32x32x16_bf16((a), (b), (c), 0, 0, 0)
#define SB() __builtin_amdgcn_sched_barrier(0)

constexpr int S = 2048, DM = 448, NH = 16, DH = 28;
constexpr float SCALE = 0.18898223650461362f;
constexpr float LOG2E = 1.4426950408889634f;
constexpr float QSCALE = SCALE * LOG2E;

constexpr size_t WS_Q   = 0;
constexpr size_t WS_KV  = WS_Q + (size_t)NH * S * 32 * 2;
constexpr size_t WS_KN  = WS_KV + (size_t)64 * 4096;
constexpr size_t WS_QKV = WS_KN + 256;
constexpr size_t WS_O   = WS_QKV + (size_t)S * 1344 * 2 + 8192;

DI unsigned cvtpk(float lo, float hi) { f32x2_t v = {lo, hi}; return __builtin_bit_cast(unsigned, __builtin_convertvector(v, bf16x2_t)); }
DI void glds16(const void* g, unsigned lds_base) {
  unsigned sv; asm volatile("s_mov_b32 %0, m0\n\ts_mov_b32 m0, %2\n\ts_nop 0\n\tglobal_load_lds_dwordx4 %1, off\n\ts_mov_b32 m0, %0"
                            : "=&s"(sv) : "v"(g), "s"(lds_base) : "memory"); }
template <int N> DI void wait_bar() { asm volatile("s_waitcnt vmcnt(%0) lgkmcnt(0)\n\ts_barrier" :: "i"(N) : "memory"); }
DI bf16x8 lds_rd128(lds_cptr p) { return *(const __attribute__((address_space(3))) bf16x8*)p; }
DI s16x4 lds_tr(lds_cptr p) { return __builtin_bit_cast(s16x4, __builtin_amdgcn_ds_read_tr16_b64_v4i16((__attribute__((address_space(3))) v4i16_t*)p)); }
DI unsigned sfl(unsigned x) { return (unsigned)__builtin_amdgcn_readfirstlane((int)x); }
template <int I> struct IC { static constexpr int value = I; };
template <int I, int N, class F> DI void static_for(F&& f) { if constexpr (I < N) { f(IC<I>{}); static_for<I + 1, N>(f); } }
DI float bflo(unsigned u) { return __uint_as_float(u << 16); }
DI float bfhi(unsigned u) { return __uint_as_float(u & 0xffff0000u); }

constexpr int G_NBUF = 4;
template <int NPP> struct GemmCfg { static constexpr int STAGE_BYTES = 16384 + NPP * 8192; static constexpr int RING = STAGE_BYTES * G_NBUF; };
constexpr int K1_LDS = GemmCfg<1>::RING + 2 * 448 * 64;
static_assert(GemmCfg<2>::RING <= K1_LDS, "");
constexpr int K3_LDS = GemmCfg<1>::RING;

template <int MODE, int NPP>
DI void gemm_tile(char* lds, const u16* __restrict__ X, int ldx, int row0, const u16* __restrict__ W, int ldw, int col0, int ncols,
                  const u16* __restrict__ Wk, const u16* __restrict__ Wv, u16* __restrict__ out0, u16* __restrict__ out1) {
  constexpr int SBY = GemmCfg<NPP>::STAGE_BYTES;
  constexpr int NPW = (MODE == 1) ? 2 : NPP;
  constexpr int L = 2 + NPP;
  const int tid = threadIdx.x, lane = tid & 63, r32 = lane & 31, hi = lane >> 5;
  const int wid = (int)sfl(tid >> 6);
  const unsigned lds0 = (unsigned)(uintptr_t)lds;
  const u16* xsrc[2];
#pragma unroll
  for (int i = 0; i < 2; ++i) { const int row = (2 * wid + i) * 8 + (lane >> 3); const int c = (lane & 7) ^ ((row >> 1) & 7);
    xsrc[i] = X + (size_t)(row0 + row) * ldx + c * 8; }
  const u16* wsrc[NPP];
#pragma unroll
  for (int i = 0; i < NPP; ++i) { int pc = col0 + i * 64; if (pc > ncols - 64) pc = ncols - 64;
    const int k = wid * 8 + (lane >> 3); const int c = (lane & 7) ^ (((k >> 1) & 1) << 2);
    wsrc[i] = W + (size_t)k * ldw + pc + c * 8; }

  auto issue = [&](int s) {
    const unsigned base = lds0 + (unsigned)((s % G_NBUF) * SBY);
    glds16(xsrc[0] + s * 64, sfl(base + (2 * wid) * 1024));
    glds16(xsrc[1] + s * 64, sfl(base + (2 * wid + 1) * 1024));
#pragma unroll
    for (int i = 0; i < NPP; ++i) glds16(wsrc[i] + (size_t)s * 64 * ldw, sfl(base + 16384 + i * 8192 + wid * 1024));
  };

  const int wr = wid & 3, wc = wid >> 2;
  const int xrow = wr * 32 + r32; const int xsw = (xrow >> 1) & 7;
  const int g16 = lane >> 4, i16 = lane & 15, tq = i16 >> 2;
  const int toff128 = (8 * (g16 >> 1) + tq) * 128 + (g16 & 1) * 32 + (i16 & 3) * 8; const int tsw = (tq >> 1) & 1;
  const int toff64  = (8 * (g16 >> 1) + tq) * 64 + (g16 & 1) * 32 + (i16 & 3) * 8;

  f32x16 acc[NPW];
#pragma unroll
  for (int p = 0; p < NPW; ++p) acc[p] = f32x16{};

  u32x2 kvreg[13];
  if constexpr (MODE == 1) {
#pragma unroll
    for (int i = 0; i < 13; ++i) { int e = tid + i * 512; if (e > 6271) e = 6271; const int m = e >= 3136; const int e2 = e - m * 3136;
      kvreg[i] = *(const u32x2*)((m ? Wv : Wk) + e2 * 4); }
  }
  issue(0); issue(1); issue(2);
  if constexpr (MODE == 1) {
#pragma unroll
    for (int i = 0; i < 13; ++i) { int e = tid + i * 512; if (e > 6271) e = 6271; const int m = e >= 3136; const int e2 = e - m * 3136; const int k = e2 / 7, c = e2 - k * 7;
      *(u32x2*)(lds + GemmCfg<1>::RING + m * 28672 + k * 64 + c * 8) = kvreg[i]; }
  }
  static_for<0, 7>([&](auto sc) {
    constexpr int s = decltype(sc)::value;
    constexpr int ahead = (s + 2 < 6 ? s + 2 : 6) - s;
    wait_bar<ahead * L>();
    if constexpr (s + 3 <= 6) issue(s + 3);
    const lds_cptr b = (lds_cptr)lds + (s % G_NBUF) * SBY;
#pragma unroll
    for (int ks = 0; ks < 4; ++ks) {
      const bf16x8 xf = lds_rd128(b + xrow * 128 + (((2 * ks + hi) ^ xsw) * 16));
#pragma unroll
      for (int p = 0; p < NPW; ++p) {
        lds_cptr a; int step;
        if (MODE == 1 && wc == 1) { a = (lds_cptr)lds + GemmCfg<1>::RING + p * 28672 + (s * 64 + ks * 16) * 64 + toff64; step = 256; }
        else if (MODE == 1 || NPP == 2) { a = b + 16384 + (MODE == 1 ? 0 : wc * 8192) + ks * 2048 + toff128 + ((p ^ tsw) * 64); step = 512; }
        else { a = b + 16384 + ks * 2048 + toff128 + ((wc ^ tsw) * 64); step = 512; }
        const s16x4 lo = lds_tr(a), hh = lds_tr(a + step);
        const bf16x8 wf = __builtin_shufflevector(lo, hh, 0, 1, 2, 3, 4, 5, 6, 7);
        if (MODE == 1 && p == 1 && wc == 1) acc[p] = MFMA(xf, wf, acc[p]);
        else acc[p] = MFMA(wf, xf, acc[p]);
      }
    }
  });

  const int row = row0 + xrow;
  if constexpr (MODE >= 2) {
    constexpr int RB = NPW * 64 + 16;
    __syncthreads();
    char* st = lds + wid * (32 * RB);
#pragma unroll
    for (int p = 0; p < NPW; ++p)
#pragma unroll
      for (int g = 0; g < 4; ++g)
        *(u32x2*)(st + r32 * RB + p * 64 + g * 16 + hi * 8) = u32x2{cvtpk(acc[p][4 * g], acc[p][4 * g + 1]), cvtpk(acc[p][4 * g + 2], acc[p][4 * g + 3])};
    asm volatile("s_waitcnt lgkmcnt(0)" ::: "memory");
    constexpr int LPR = NPW * 4;
    const int ld = MODE == 2 ? 1344 : 896;
    const int cbase = col0 + wc * NPW * 32;
    if (MODE == 3 || cbase < 1344) {
#pragma unroll
      for (int i = 0; i < 32 * LPR / 64; ++i) { const int rr = i * (64 / LPR) + lane / LPR, ch = lane % LPR;
        *(u32x4*)(out0 + (size_t)(row0 + wr * 32 + rr) * ld + cbase + ch * 8) = *(const u32x4*)(st + rr * RB + ch * 16); }
    }
  } else {
#pragma unroll
    for (int p = 0; p < NPW; ++p) {
      const int panel = wc * NPW + p;
      if (MODE == 1 && panel >= 2) {
        const int tile = (row0 >> 5) + wr; u16* kv = out1 + (size_t)tile * 2048;
        if (panel == 2) {
          float ss = 0.f;
#pragma unroll
          for (int g = 0; g < 4; ++g) { u32x2 v;
            if (g == 3 && hi == 1) v = u32x2{0x3F803F80u, 0u};
            else { v = u32x2{cvtpk(acc[p][4 * g], acc[p][4 * g + 1]), cvtpk(acc[p][4 * g + 2], acc[p][4 * g + 3])};
#pragma unroll
              for (int i = 0; i < 2; ++i) { const float a = bflo(v[i]), b = bfhi(v[i]); ss = __builtin_fmaf(a, a, ss); ss = __builtin_fmaf(b, b, ss); } }
            *(u32x2*)(kv + (g >> 1) * 512 + (r32 + 32 * (g & 1)) * 8 + 4 * hi) = v; }
          ss += __shfl_xor(ss, 32, 64);
#pragma unroll
          for (int o = 16; o > 0; o >>= 1) ss = __builtin_fmaxf(ss, __shfl_xor(ss, o, 64));
          if (lane == 0) ((float*)((char*)out1 + (WS_KN - WS_KV)))[tile] = ss;
        } else {
#pragma unroll
          for (int g = 0; g < 4; ++g) { u32x2 v = u32x2{cvtpk(acc[p][4 * g], acc[p][4 * g + 1]), cvtpk(acc[p][4 * g + 2], acc[p][4 * g + 3])};
            if (r32 == 28) v = u32x2{0x3F803F80u, 0x3F803F80u}; else if (r32 > 28) v = u32x2{0u, 0u};
            *(u32x2*)(kv + 1024 + (g >> 1) * 512 + lane * 8 + 4 * (g & 1)) = v; }
        }
      } else {
#pragma unroll
        for (int g = 0; g < 4; ++g) { const int c = col0 + panel * 32 + 8 * g + 4 * hi; const int head = c / 28, d = c - head * 28;
          u32x2 v = u32x2{cvtpk(acc[p][4 * g] * QSCALE, acc[p][4 * g + 1] * QSCALE), cvtpk(acc[p][4 * g + 2] * QSCALE, acc[p][4 * g + 3] * QSCALE)};
          u16* dst = out0 + ((size_t)head * S + row) * 32 + d;
          if (d == 24) *(u32x4*)dst = u32x4{v[0], v[1], 0u, 0u}; else *(u32x2*)dst = v; }
      }
    }
  }
}

__global__ __launch_bounds__(512, 2) void k1_proj(const u16* __restrict__ Ax, const u16* __restrict__ Awq, const u16* __restrict__ Awk, const u16* __restrict__ Awv,
                                                  const u16* __restrict__ Bx, const u16* __restrict__ Bw, char* __restrict__ ws) {
  extern __shared__ __attribute__((aligned(16))) char lds[];
  const int b = blockIdx.x, xcd = b & 7, j = b >> 3; const int rt = 2 * xcd + (j & 1), ct = j >> 1; const int row0 = rt * 128;
  u16* Qs = (u16*)(ws + WS_Q); u16* KV = (u16*)(ws + WS_KV); u16* QKV = (u16*)(ws + WS_QKV);
  if (ct == 0) gemm_tile<1, 1>(lds, Ax, DM, row0, Awq, DM, 384, DM, Awk, Awv, Qs, KV);
  else if (ct < 4) gemm_tile<0, 2>(lds, Ax, DM, row0, Awq, DM, (ct - 1) * 128, DM, nullptr, nullptr, Qs, nullptr);
  else gemm_tile<2, 2>(lds, Bx, DM, row0, Bw, 1344, (ct - 4) * 128, 1344, nullptr, nullptr, QKV, nullptr);
}

__global__ __launch_bounds__(512, 2) void k3_out(const char* __restrict__ ws, const u16* __restrict__ Awo, const u16* __restrict__ Bwo, u16* __restrict__ out) {
  extern __shared__ __attribute__((aligned(16))) char lds[];
  const int b = blockIdx.x, xcd = b & 7, j = b >> 3; const int rt = 2 * xcd + (j & 1), ct = j >> 1; const int row0 = rt * 128;
  const u16* O = (const u16*)(ws + WS_O);
  const int part = ct >= 7 ? 1 : 0; const int c0 = (ct - 7 * part) * 64;
  gemm_tile<3, 1>(lds, O + part * 448, 896, row0, part ? Bwo : Awo, DM, c0, DM, nullptr, nullptr, out + part * 448, nullptr);
}

constexpr int A_STAGE = 16384;
constexpr int A_LDS_SIDE = 4 * A_STAGE;
constexpr int A_LDS_MRG = A_LDS_SIDE + 65536;
constexpr int A_LDS = A_LDS_MRG + 4 * 17 * 256;
constexpr float THR = 30.f;

DI float max16(const f32x16& s) {
  float a = __builtin_fmaxf(__builtin_fmaxf(s[0], s[1]), s[2]), b = __builtin_fmaxf(__builtin_fmaxf(s[3], s[4]), s[5]);
  a = __builtin_fmaxf(__builtin_fmaxf(a, s[6]), s[7]); b = __builtin_fmaxf(__builtin_fmaxf(b, s[8]), s[9]);
  a = __builtin_fmaxf(__builtin_fmaxf(a, s[10]), s[11]); b = __builtin_fmaxf(__builtin_fmaxf(b, s[12]), s[13]);
  a = __builtin_fmaxf(__builtin_fmaxf(a, s[14]), s[15]);
  return __builtin_fmaxf(a, b); }
DI float xhalf_max(float m) { auto rr = __builtin_amdgcn_permlane32_swap(__float_as_uint(m), __float_as_uint(m), false, false);
  return __builtin_fmaxf(__uint_as_float(rr[0]), __uint_as_float(rr[1])); }
DI float bf_round(float x) { return __uint_as_float(cvtpk(x, 0.f) << 16); }

__global__ __launch_bounds__(512, 2) void k2_attn(char* __restrict__ ws) {
  extern __shared__ __attribute__((aligned(16))) char lds[];
  const int tid = threadIdx.x, lane = tid & 63, r32 = lane & 31, hi = lane >> 5;
  const int wid = (int)sfl(tid >> 6), qg = wid & 3, kh = wid >> 2;
  const int b = blockIdx.x, xcd = b & 7, j = b >> 3;
  const int rc = xcd * 8 + (j >> 2), hg = j & 3;
  const int head = hg * 4 + qg, row0 = rc * 32;
  const u16* Qs = (const u16*)(ws + WS_Q); const char* KV = ws + WS_KV; const char* QKV = ws + WS_QKV; u16* O = (u16*)(ws + WS_O);
  const unsigned lds0 = (unsigned)(uintptr_t)lds;

  const char* kvsrc = KV + (size_t)(kh * 32) * 4096 + (2 * qg) * 1024 + lane * 16;
  const unsigned kvdst = kh * 8192 + (2 * qg) * 1024;
  auto issue_kv = [&](int s) { const unsigned d = sfl(lds0 + (s & 3) * A_STAGE + kvdst);
    glds16(kvsrc + (size_t)s * 8192, d); glds16(kvsrc + (size_t)s * 8192 + 1024, d + 1024); };
  const int brow0 = (xcd * 32 + j) * 8; const int bstart = brow0 >= 14 ? brow0 - 14 : 0;
  const char* sdsrc = QKV + (size_t)bstart * 2688 + wid * 1024 + lane * 16;
  auto issue_side = [&](int s) { glds16(sdsrc + s * 8192, sfl(lds0 + A_LDS_SIDE + s * 8192 + wid * 1024)); };

  const u16* qp = Qs + ((size_t)head * S + row0 + r32) * 32 + hi * 8;
  const bf16x8 q0 = *(const bf16x8*)qp; u32x4 q1 = *(const u32x4*)(qp + 16);
  float kn2 = ((const float*)(ws + WS_KN))[lane];
  issue_kv(0); issue_kv(1); issue_kv(2); issue_kv(3);

  float qn2 = 0.f;
  { const u32x4 a = __builtin_bit_cast(u32x4, q0);
#pragma unroll
    for (int i = 0; i < 4; ++i) { float x = bflo(a[i]), y = bfhi(a[i]); qn2 = __builtin_fmaf(x, x, qn2); qn2 = __builtin_fmaf(y, y, qn2);
      x = bflo(q1[i]); y = bfhi(q1[i]); qn2 = __builtin_fmaf(x, x, qn2); qn2 = __builtin_fmaf(y, y, qn2); } }
  qn2 += __shfl_xor(qn2, 32, 64);
#pragma unroll
  for (int o = 32; o > 0; o >>= 1) kn2 = __builtin_fmaxf(kn2, __shfl_xor(kn2, o, 64));
  const float bound = __builtin_sqrtf(qn2 * kn2) * 1.01f + 1.0f;

  f32x16 o = f32x16{};
  const lds_cptr rd0 = (lds_cptr)lds + kh * 8192 + lane * 16;
  float mref, m0;
  auto set_ref = [&](float m) {
    const float mh = bf_round(m), ml = bf_round(m - mh); mref = mh + ml;
    const unsigned pk = cvtpk(-mh, -ml); if (hi) q1[2] = pk; };
#define Q1 __builtin_bit_cast(bf16x8, q1)
#define EXPK(S_, i) cvtpk(__builtin_amdgcn_exp2f(S_[i]), __builtin_amdgcn_exp2f(S_[i + 1]))
#define STAGE_SYNC(s_) do { if ((s_) < 14) { wait_bar<2>(); if ((s_) <= 8) issue_side((s_) - 1); if ((s_) <= 12) issue_kv((s_) + 3); } else wait_bar<0>(); } while (0)

  wait_bar<4>();
  bf16x8 ka0 = lds_rd128(rd0), ka1 = lds_rd128(rd0 + 1024);
  { f32x16 s = MFMA(ka0, q0, f32x16{}); s = MFMA(ka1, Q1, s); m0 = xhalf_max(max16(s)); }
  const bool fast = __all(bound - m0 <= 198.f);
  set_ref(fast ? __builtin_fmaxf(m0, bound - 108.f) : m0);

  if (fast) {
    f32x16 sc, sn; bf16x8 kb0, kb1, va0, va1, vb0, vb1;
    sc = MFMA(ka0, q0, f32x16{}); sc = MFMA(ka1, Q1, sc);
    ka0 = lds_rd128(rd0 + 4096); ka1 = lds_rd128(rd0 + 4096 + 1024); va0 = lds_rd128(rd0 + 2048); va1 = lds_rd128(rd0 + 3072);
#define ITER(SC_, SN_, K0, K1, V0, V1, NK0, NK1, NV0, NV1, PK_, PV_, HASK, HASV, HASN) do { \
      if (HASK) { NK0 = lds_rd128(PK_); NK1 = lds_rd128((PK_) + 1024); } \
      if (HASV) { NV0 = lds_rd128((PV_) + 2048); NV1 = lds_rd128((PV_) + 3072); } SB(); \
      u32x4 p0, p1; \
      p0[0] = EXPK(SC_, 0); p0[1] = EXPK(SC_, 2); SB(); \
      if (HASN) SN_ = MFMA(K0, q0, f32x16{}); SB(); \
      p0[2] = EXPK(SC_, 4); p0[3] = EXPK(SC_, 6); SB(); \
      o = MFMA(V0, __builtin_bit_cast(bf16x8, p0), o); SB(); \
      p1[0] = EXPK(SC_, 8); p1[1] = EXPK(SC_, 10); SB(); \
      if (HASN) SN_ = MFMA(K1, Q1, SN_); SB(); \
      p1[2] = EXPK(SC_, 12); p1[3] = EXPK(SC_, 14); SB(); \
      o = MFMA(V1, __builtin_bit_cast(bf16x8, p1), o); SB(); } while (0)
    for (int s = 0; s < 15; ++s) {
      const lds_cptr p = rd0 + (s & 3) * A_STAGE, pn = rd0 + ((s + 1) & 3) * A_STAGE;
      if (s > 0) STAGE_SYNC(s);
      ITER(sc, sn, ka0, ka1, va0, va1, kb0, kb1, vb0, vb1, pn, p + 4096, true, true, true);
      ITER(sn, sc, kb0, kb1, vb0, vb1, ka0, ka1, va0, va1, pn + 4096, pn, true, true, true);
    }
    { const lds_cptr p = rd0 + 3 * A_STAGE;
      ITER(sc, sn, ka0, ka1, va0, va1, kb0, kb1, vb0, vb1, p, p + 4096, false, true, true);
      ITER(sn, sc, kb0, kb1, vb0, vb1, ka0, ka1, va0, va1, p, p, false, false, false); }
  } else {
    auto tile = [&](lds_cptr p) {
      const bf16x8 kf0 = lds_rd128(p), kf1 = lds_rd128(p + 1024), vf0 = lds_rd128(p + 2048), vf1 = lds_rd128(p + 3072);
      f32x16 s = MFMA(kf0, q0, f32x16{}); s = MFMA(kf1, Q1, s);
      const float tm = max16(s);
      if (__any(tm > THR)) {
        const float old = mref; set_ref(mref + __builtin_fmaxf(xhalf_max(tm), 0.f)); const float dl = mref - old; const float al = __builtin_amdgcn_exp2f(-dl);
#pragma unroll
        for (int r = 0; r < 16; ++r) { s[r] -= dl; o[r] *= al; }
      }
      u32x4 p0, p1;
#pragma unroll
      for (int i = 0; i < 4; ++i) { p0[i] = EXPK(s, 2 * i); p1[i] = EXPK(s, 8 + 2 * i); }
      o = MFMA(vf0, __builtin_bit_cast(bf16x8, p0), o); o = MFMA(vf1, __builtin_bit_cast(bf16x8, p1), o);
    };
    for (int s = 0; s < 16; ++s) {
      const lds_cptr p = rd0 + (s & 3) * A_STAGE;
      if (s > 0 && s < 15) STAGE_SYNC(s);
      tile(p); tile(p + 4096);
    }
  }

  float* mrg = (float*)(lds + A_LDS_MRG) + qg * 17 * 64;
  if (kh == 1) {
#pragma unroll
    for (int r = 0; r < 16; ++r) mrg[r * 64 + lane] = o[r];
    mrg[16 * 64 + lane] = -mref;
  }
  __syncthreads();
  if (kh == 0) {
    const float nb = mrg[16 * 64 + lane], na = -mref;
    const float nm = __builtin_fminf(na, nb);
    const float fa = __builtin_amdgcn_exp2f(nm - na), fb = __builtin_amdgcn_exp2f(nm - nb);
#pragma unroll
    for (int r = 0; r < 16; ++r) o[r] = o[r] * fa + mrg[r * 64 + lane] * fb;
    const float l = __shfl(o[12], r32 + 32, 64);
    const float il = 1.0f / l;
    u16* dst = O + (size_t)(row0 + r32) * 896 + head * 28 + 4 * hi;
#pragma unroll
    for (int g = 0; g < 4; ++g) if (!(g == 3 && hi == 1))
      *(u32x2*)(dst + 8 * g) = u32x2{cvtpk(o[4 * g] * il, o[4 * g + 1] * il), cvtpk(o[4 * g + 2] * il, o[4 * g + 3] * il)};
  }
  {
    const int pr = tid >> 2, wq = tid & 3, rl = pr >> 4, hb = pr & 15; const int r = brow0 + rl;
    const char* img = lds + A_LDS_SIDE + hb * 56;
    const unsigned* qv = (const unsigned*)(img + (r - bstart) * 2688);
    unsigned qu[14];
#pragma unroll
    for (int i = 0; i < 14; ++i) qu[i] = qv[i];
    float lg[2]; const unsigned* vp[2];
#pragma unroll
    for (int e = 0; e < 2; ++e) { const int w = 2 * wq + e; const int jr = r - 2 * w; const int jc = jr >= 0 ? jr : bstart;
      const unsigned* kp = (const unsigned*)(img + (jc - bstart) * 2688 + 896); vp[e] = kp + 224; float a = 0.f, a2 = 0.f;
#pragma unroll
      for (int i = 0; i < 14; ++i) { const unsigned u = kp[i]; a = __builtin_fmaf(bflo(qu[i]), bflo(u), a); a2 = __builtin_fmaf(bfhi(qu[i]), bfhi(u), a2); }
      lg[e] = jr >= 0 ? (a + a2) * SCALE : -1e9f; }
    float m = __builtin_fmaxf(lg[0], lg[1]); m = __builtin_fmaxf(m, __shfl_xor(m, 1, 64)); m = __builtin_fmaxf(m, __shfl_xor(m, 2, 64));
    float e0 = __builtin_amdgcn_exp2f((lg[0] - m) * LOG2E), e1 = __builtin_amdgcn_exp2f((lg[1] - m) * LOG2E);
    float l = e0 + e1; l += __shfl_xor(l, 1, 64); l += __shfl_xor(l, 2, 64);
    const float il = 1.0f / l; e0 *= il; e1 *= il;
    float oa[28];
#pragma unroll
    for (int i = 0; i < 14; ++i) { const unsigned u0 = vp[0][i], u1 = vp[1][i];
      oa[2 * i] = __builtin_fmaf(e0, bflo(u0), e1 * bflo(u1)); oa[2 * i + 1] = __builtin_fmaf(e0, bfhi(u0), e1 * bfhi(u1)); }
#pragma unroll
    for (int i = 0; i < 28; ++i) { oa[i] += __shfl_xor(oa[i], 1, 64); oa[i] += __shfl_xor(oa[i], 2, 64); }
    unsigned* dst = (unsigned*)(O + (size_t)r * 896 + 448 + hb * 28);
#pragma unroll
    for (int i = 0; i < 14; ++i) if ((i >> 2) == wq) dst[i] = cvtpk(oa[2 * i], oa[2 * i + 1]);
  }
}

extern "C" void kernel_launch(void* const* d_in, const int* in_sizes, int n_in, void* d_out, int out_size, void* d_ws, size_t ws_size, hipStream_t stream) {
  const u16 *Ax = (const u16*)d_in[0], *Awq = (const u16*)d_in[1], *Awk = (const u16*)d_in[2], *Awv = (const u16*)d_in[3], *Awo = (const u16*)d_in[4];
  const u16 *Bx = (const u16*)d_in[5], *Bw = (const u16*)d_in[6], *Bwo = (const u16*)d_in[7];
  char* ws = (char*)d_ws;
  (void)hipFuncSetAttribute((const void*)k1_proj, hipFuncAttributeMaxDynamicSharedMemorySize, K1_LDS);
  (void)hipFuncSetAttribute((const void*)k2_attn, hipFuncAttributeMaxDynamicSharedMemorySize, A_LDS);
  (void)hipFuncSetAttribute((const void*)k3_out, hipFuncAttributeMaxDynamicSharedMemorySize, K3_LDS);
  k1_proj<<<240, 512, K1_LDS, stream>>>(Ax, Awq, Awk, Awv, Bx, Bw, ws);
  k2_attn<<<256, 512, A_LDS, stream>>>(ws);
  k3_out<<<224, 512, K3_LDS, stream>>>(ws, Awo, Bwo, (u16*)d_out);
}
```

```cpp
#include <hip/hip_runtime.h>
#include <stdint.h>

typedef unsigned short u16;
using bf16x8 = __attribute__((ext_vector_type(8))) short;
using s16x4  = __attribute__((ext_vector_type(4))) short;
using f32x16 = __attribute__((ext_vector_type(16))) float;
using u32x4  = __attribute__((ext_vector_type(4))) unsigned;
using u32x2  = __attribute__((ext_vector_type(2))) unsigned;
typedef __attribute__((address_space(3))) const char* lds_cptr;
typedef __attribute__((address_space(3))) char* lds_ptr;
typedef short v4i16_t __attribute__((ext_vector_type(4)));
typedef __bf16 bf16x2_t __attribute__((ext_vector_type(2)));
typedef float f32x2_t __attribute__((ext_vector_type(2)));
#define DI __device__ __forceinline__
#define MFMA(a, b, c) __builtin_amdgcn_mfma_f32_32x32x16_bf16((a), (b), (c), 0, 0, 0)
#define SB() __builtin_amdgcn_sched_barrier(0)

constexpr int S = 2048, DM = 448, NH = 16, DH = 28;
constexpr float SCALE = 0.18898223650461362f;
constexpr float LOG2E = 1.4426950408889634f;
constexpr float QSCALE = SCALE * LOG2E;

constexpr size_t WS_Q   = 0;
constexpr size_t WS_KV  = WS_Q + (size_t)NH * S * 32 * 2;
constexpr size_t WS_KN  = WS_KV + (size_t)64 * 4096;
constexpr size_t WS_QKV = WS_KN + 256;
constexpr size_t WS_O   = WS_QKV + (size_t)S * 1344 * 2 + 8192;

DI unsigned cvtpk(float lo, float hi) { f32x2_t v = {lo, hi}; return __builtin_bit_cast(unsigned, __builtin_convertvector(v, bf16x2_t)); }
DI void glds16(const void* g, unsigned lds_base) {
  unsigned sv; asm volatile("s_mov_b32 %0, m0\n\ts_mov_b32 m0, %2\n\ts_nop 0\n\tglobal_load_lds_dwordx4 %1, off\n\ts_mov_b32 m0, %0"
                            : "=&s"(sv) : "v"(g), "s"(lds_base) : "memory"); }
template <int N> DI void wait_bar() { asm volatile("s_waitcnt vmcnt(%0) lgkmcnt(0)\n\ts_barrier" :: "i"(N) : "memory"); }
DI bf16x8 lds_rd128(lds_cptr p) { return *(const __attribute__((address_space(3))) bf16x8*)p; }
DI s16x4 lds_tr(lds_cptr p) { return __builtin_bit_cast(s16x4, __builtin_amdgcn_ds_read_tr16_b64_v4i16((__attribute__((address_space(3))) v4i16_t*)p)); }
DI unsigned sfl(unsigned x) { return (unsigned)__builtin_amdgcn_readfirstlane((int)x); }
template <int I> struct IC { static constexpr int value = I; };
template <int I, int N, class F> DI void static_for(F&& f) { if constexpr (I < N) { f(IC<I>{}); static_for<I + 1, N>(f); } }
DI float bflo(unsigned u) { return __uint_as_float(u << 16); }
DI float bfhi(unsigned u) { return __uint_as_float(u & 0xffff0000u); }

constexpr int G_NBUF = 4;
template <int NPP> struct GemmCfg { static constexpr int STAGE_BYTES = 16384 + NPP * 8192; static constexpr int RING = STAGE_BYTES * G_NBUF; };
constexpr int K1_LDS = GemmCfg<2>::RING;
static_assert(GemmCfg<0>::RING + 2 * 448 * 64 <= K1_LDS, "");
constexpr int K3_LDS = GemmCfg<1>::RING;

template <int MODE, int NPP>
DI void gemm_tile(char* lds, const u16* __restrict__ X, int ldx, int row0, const u16* __restrict__ W, int ldw, int col0, int ncols,
                  const u16* __restrict__ Wk, const u16* __restrict__ Wv, u16* __restrict__ out0, u16* __restrict__ out1) {
  constexpr int SBY = GemmCfg<NPP>::STAGE_BYTES;
  constexpr int NPW = (MODE == 1) ? 1 : NPP;
  constexpr int L = 2 + NPP;
  const int tid = threadIdx.x, lane = tid & 63, r32 = lane & 31, hi = lane >> 5;
  const int wid = (int)sfl(tid >> 6);
  const unsigned lds0 = (unsigned)(uintptr_t)lds;
  const u16* xsrc[2];
#pragma unroll
  for (int i = 0; i < 2; ++i) { const int row = (2 * wid + i) * 8 + (lane >> 3); const int c = (lane & 7) ^ ((row >> 1) & 7);
    xsrc[i] = X + (size_t)(row0 + row) * ldx + c * 8; }
  const u16* wsrc[NPP > 0 ? NPP : 1];
#pragma unroll
  for (int i = 0; i < NPP; ++i) { int pc = col0 + i * 64; if (pc > ncols - 64) pc = ncols - 64;
    const int k = wid * 8 + (lane >> 3); const int c = (lane & 7) ^ (((k >> 1) & 1) << 2);
    wsrc[i] = W + (size_t)k * ldw + pc + c * 8; }

  auto issue = [&](int s) {
    const unsigned base = lds0 + (unsigned)((s % G_NBUF) * SBY);
    glds16(xsrc[0] + s * 64, sfl(base + (2 * wid) * 1024));
    glds16(xsrc[1] + s * 64, sfl(base + (2 * wid + 1) * 1024));
#pragma unroll
    for (int i = 0; i < NPP; ++i) glds16(wsrc[i] + (size_t)s * 64 * ldw, sfl(base + 16384 + i * 8192 + wid * 1024));
  };

  const int wr = wid & 3, wc = wid >> 2;
  const int xrow = wr * 32 + r32; const int xsw = (xrow >> 1) & 7;
  const int g16 = lane >> 4, i16 = lane & 15, tq = i16 >> 2;
  const int toff128 = (8 * (g16 >> 1) + tq) * 128 + (g16 & 1) * 32 + (i16 & 3) * 8; const int tsw = (tq >> 1) & 1;
  const int toff64  = (8 * (g16 >> 1) + tq) * 64 + (g16 & 1) * 32 + (i16 & 3) * 8;

  f32x16 acc[NPW];
#pragma unroll
  for (int p = 0; p < NPW; ++p) acc[p] = f32x16{};

  u32x2 kvreg[13];
  if constexpr (MODE == 1) {
#pragma unroll
    for (int i = 0; i < 13; ++i) { int e = tid + i * 512; if (e > 6271) e = 6271; const int m = e >= 3136; const int e2 = e - m * 3136;
      kvreg[i] = *(const u32x2*)((m ? Wv : Wk) + e2 * 4); }
  }
  issue(0); issue(1); issue(2);
  if constexpr (MODE == 1) {
#pragma unroll
    for (int i = 0; i < 13; ++i) { int e = tid + i * 512; if (e > 6271) e = 6271; const int m = e >= 3136; const int e2 = e - m * 3136; const int k = e2 / 7, c = e2 - k * 7;
      *(u32x2*)(lds + GemmCfg<0>::RING + m * 28672 + k * 64 + c * 8) = kvreg[i]; }
  }
  static_for<0, 7>([&](auto sc) {
    constexpr int s = decltype(sc)::value;
    constexpr int ahead = (s + 2 < 6 ? s + 2 : 6) - s;
    wait_bar<ahead * L>();
    if constexpr (s + 3 <= 6) issue(s + 3);
    const lds_cptr b = (lds_cptr)lds + (s % G_NBUF) * SBY;
#pragma unroll
    for (int ks = 0; ks < 4; ++ks) {
      const bf16x8 xf = lds_rd128(b + xrow * 128 + (((2 * ks + hi) ^ xsw) * 16));
#pragma unroll
      for (int p = 0; p < NPW; ++p) {
        lds_cptr a; int step;
        if constexpr (MODE == 1) { a = (lds_cptr)lds + GemmCfg<0>::RING + wc * 28672 + (s * 64 + ks * 16) * 64 + toff64; step = 256; }
        else if constexpr (NPP == 2) { a = b + 16384 + wc * 8192 + ks * 2048 + toff128 + ((p ^ tsw) * 64); step = 512; }
        else { a = b + 16384 + ks * 2048 + toff128 + ((wc ^ tsw) * 64); step = 512; }
        const s16x4 lo = lds_tr(a), hh = lds_tr(a + step);
        const bf16x8 wf = __builtin_shufflevector(lo, hh, 0, 1, 2, 3, 4, 5, 6, 7);
        if constexpr (MODE == 1) { const bf16x8 fa = wc ? xf : wf, fb = wc ? wf : xf; acc[p] = MFMA(fa, fb, acc[p]); }
        else acc[p] = MFMA(wf, xf, acc[p]);
      }
    }
  });

  const int row = row0 + xrow;
  if constexpr (MODE >= 2) {
    constexpr int RB = NPW * 64 + 16;
    __syncthreads();
    char* st = lds + wid * (32 * RB);
#pragma unroll
    for (int p = 0; p < NPW; ++p)
#pragma unroll
      for (int g = 0; g < 4; ++g)
        *(u32x2*)(st + r32 * RB + p * 64 + g * 16 + hi * 8) = u32x2{cvtpk(acc[p][4 * g], acc[p][4 * g + 1]), cvtpk(acc[p][4 * g + 2], acc[p][4 * g + 3])};
    asm volatile("s_waitcnt lgkmcnt(0)" ::: "memory");
    constexpr int LPR = NPW * 4;
    const int ld = MODE == 2 ? 1344 : 896;
    const int cbase = col0 + wc * NPW * 32;
    if (MODE == 3 || cbase < 1344) {
#pragma unroll
      for (int i = 0; i < 32 * LPR / 64; ++i) { const int rr = i * (64 / LPR) + lane / LPR, ch = lane % LPR;
        *(u32x4*)(out0 + (size_t)(row0 + wr * 32 + rr) * ld + cbase + ch * 8) = *(const u32x4*)(st + rr * RB + ch * 16); }
    }
  } else {
#pragma unroll
    for (int p = 0; p < NPW; ++p) {
      const int panel = wc * NPW + p;
      if (MODE == 1) {
        const int tile = (row0 >> 5) + wr; u16* kv = out1 + (size_t)tile * 2048;
        if (wc == 0) {
          float ss = 0.f;
#pragma unroll
          for (int g = 0; g < 4; ++g) { u32x2 v;
            if (g == 3 && hi == 1) v = u32x2{0x3F803F80u, 0u};
            else { v = u32x2{cvtpk(acc[p][4 * g], acc[p][4 * g + 1]), cvtpk(acc[p][4 * g + 2], acc[p][4 * g + 3])};
#pragma unroll
              for (int i = 0; i < 2; ++i) { const float a = bflo(v[i]), b = bfhi(v[i]); ss = __builtin_fmaf(a, a, ss); ss = __builtin_fmaf(b, b, ss); } }
            *(u32x2*)(kv + (g >> 1) * 512 + (r32 + 32 * (g & 1)) * 8 + 4 * hi) = v; }
          ss += __shfl_xor(ss, 32, 64);
#pragma unroll
          for (int o = 16; o > 0; o >>= 1) ss = __builtin_fmaxf(ss, __shfl_xor(ss, o, 64));
          if (lane == 0) ((float*)((char*)out1 + (WS_KN - WS_KV)))[tile] = ss;
        } else {
#pragma unroll
          for (int g = 0; g < 4; ++g) { u32x2 v = u32x2{cvtpk(acc[p][4 * g], acc[p][4 * g + 1]), cvtpk(acc[p][4 * g + 2], acc[p][4 * g + 3])};
            if (r32 == 28) v = u32x2{0x3F803F80u, 0x3F803F80u}; else if (r32 > 28) v = u32x2{0u, 0u};
            *(u32x2*)(kv + 1024 + (g >> 1) * 512 + lane * 8 + 4 * (g & 1)) = v; }
        }
      } else {
#pragma unroll
        for (int g = 0; g < 4; ++g) { const int c = col0 + panel * 32 + 8 * g + 4 * hi; const int head = c / 28, d = c - head * 28;
          u32x2 v = u32x2{cvtpk(acc[p][4 * g] * QSCALE, acc[p][4 * g + 1] * QSCALE), cvtpk(acc[p][4 * g + 2] * QSCALE, acc[p][4 * g + 3] * QSCALE)};
          u16* dst = out0 + ((size_t)head * S + row) * 32 + d;
          if (d == 24) *(u32x4*)dst = u32x4{v[0], v[1], 0u, 0u}; else *(u32x2*)dst = v; }
      }
    }
  }
}

__global__ __launch_bounds__(512, 2) void k1_proj(const u16* __restrict__ Ax, const u16* __restrict__ Awq, const u16* __restrict__ Awk, const u16* __restrict__ Awv,
                                                  const u16* __restrict__ Bx, const u16* __restrict__ Bw, char* __restrict__ ws) {
  extern __shared__ __attribute__((aligned(16))) char lds[];
  const int b = blockIdx.x, xcd = b & 7, j = b >> 3; const int rt = 2 * xcd + (j & 1), ct = j >> 1; const int row0 = rt * 128;
  u16* Qs = (u16*)(ws + WS_Q); u16* KV = (u16*)(ws + WS_KV); u16* QKV = (u16*)(ws + WS_QKV);
  if (ct >= 5) gemm_tile<2, 2>(lds, Bx, DM, row0, Bw, 1344, (ct - 5) * 128, 1344, nullptr, nullptr, QKV, nullptr);
  else if (ct >= 2) gemm_tile<0, 2>(lds, Ax, DM, row0, Awq, DM, (ct - 2) * 128, DM, nullptr, nullptr, Qs, nullptr);
  else if (ct == 1) gemm_tile<0, 1>(lds, Ax, DM, row0, Awq, DM, 384, DM, nullptr, nullptr, Qs, nullptr);
  else gemm_tile<1, 0>(lds, Ax, DM, row0, Awq, DM, 0, DM, Awk, Awv, Qs, KV);
}

__global__ __launch_bounds__(512, 2) void k3_out(const char* __restrict__ ws, const u16* __restrict__ Awo, const u16* __restrict__ Bwo, u16* __restrict__ out) {
  extern __shared__ __attribute__((aligned(16))) char lds[];
  const int b = blockIdx.x, xcd = b & 7, j = b >> 3; const int rt = 2 * xcd + (j & 1), ct = j >> 1; const int row0 = rt * 128;
  const u16* O = (const u16*)(ws + WS_O);
  const int part = ct >= 7 ? 1 : 0; const int c0 = (ct - 7 * part) * 64;
  gemm_tile<3, 1>(lds, O + part * 448, 896, row0, part ? Bwo : Awo, DM, c0, DM, nullptr, nullptr, out + part * 448, nullptr);
}

constexpr int A_STAGE = 16384;
constexpr int A_LDS_SIDE = 4 * A_STAGE;
constexpr int A_LDS_MRG = A_LDS_SIDE + 65536;
constexpr int A_LDS = A_LDS_MRG + 4 * 17 * 256;
constexpr float THR = 30.f;

DI float max16(const f32x16& s) {
  float a = __builtin_fmaxf(__builtin_fmaxf(s[0], s[1]), s[2]), b = __builtin_fmaxf(__builtin_fmaxf(s[3], s[4]), s[5]);
  a = __builtin_fmaxf(__builtin_fmaxf(a, s[6]), s[7]); b = __builtin_fmaxf(__builtin_fmaxf(b, s[8]), s[9]);
  a = __builtin_fmaxf(__builtin_fmaxf(a, s[10]), s[11]); b = __builtin_fmaxf(__builtin_fmaxf(b, s[12]), s[13]);
  a = __builtin_fmaxf(__builtin_fmaxf(a, s[14]), s[15]);
  return __builtin_fmaxf(a, b); }
DI float xhalf_max(float m) { auto rr = __builtin_amdgcn_permlane32_swap(__float_as_uint(m), __float_as_uint(m), false, false);
  return __builtin_fmaxf(__uint_as_float(rr[0]), __uint_as_float(rr[1])); }
DI float bf_round(float x) { return __uint_as_float(cvtpk(x, 0.f) << 16); }

DI float dot2(unsigned a, unsigned b, float c) { return __builtin_amdgcn_fdot2_f32_bf16(__builtin_bit_cast(bf16x2_t, a), __builtin_bit_cast(bf16x2_t, b), c, false); }
DI void lbar() { asm volatile("s_waitcnt lgkmcnt(0)\n\ts_barrier" ::: "memory"); }

__global__ __launch_bounds__(576, 1) void k2_attn(char* __restrict__ ws) {
  extern __shared__ __attribute__((aligned(16))) char lds[];
  const int tid = threadIdx.x, lane = tid & 63, r32 = lane & 31, hi = lane >> 5;
  const int wid = (int)sfl(tid >> 6), qg = wid & 3, kh = (wid >> 2) & 1;
  const int b = blockIdx.x, xcd = b & 7, j = b >> 3;
  const int rc = xcd * 8 + (j >> 2), hg = j & 3;
  const int head = hg * 4 + qg, row0 = rc * 32;
  const u16* Qs = (const u16*)(ws + WS_Q); const char* KV = ws + WS_KV; const char* QKV = ws + WS_QKV; u16* O = (u16*)(ws + WS_O);
  const unsigned lds0 = (unsigned)(uintptr_t)lds;
  const int brow0 = (xcd * 32 + j) * 8; const int bstart = brow0 >= 14 ? brow0 - 14 : 0;

  if (wid == 8) {
    const char* kvsrc = KV + lane * 16;
    const char* sdsrc = QKV + (size_t)bstart * 2688 + lane * 16;
    auto issue_kv = [&](int s) { const unsigned d = lds0 + (s & 3) * A_STAGE;
#pragma unroll
      for (int i = 0; i < 8; ++i) glds16(kvsrc + (size_t)s * 8192 + i * 1024, d + i * 1024);
#pragma unroll
      for (int i = 0; i < 8; ++i) glds16(kvsrc + (size_t)(s * 8192 + 131072) + i * 1024, d + 8192 + i * 1024); };
    issue_kv(0); issue_kv(1); issue_kv(2); issue_kv(3);
    wait_bar<32>();
    for (int s = 1; s < 15; ++s) {
      if (s < 14) wait_bar<16>(); else wait_bar<0>();
      if (s <= 8) {
#pragma unroll
        for (int i = 0; i < 8; ++i) glds16(sdsrc + (s - 1) * 8192 + i * 1024, lds0 + A_LDS_SIDE + (s - 1) * 8192 + i * 1024); }
      if (s <= 12) issue_kv(s + 3);
    }
    lbar();
    return;
  }

  const u16* qp = Qs + ((size_t)head * S + row0 + r32) * 32 + hi * 8;
  const bf16x8 q0 = *(const bf16x8*)qp; u32x4 q1 = *(const u32x4*)(qp + 16);
  float kn2 = ((const float*)(ws + WS_KN))[lane];

  float qn2 = 0.f;
  { const u32x4 a = __builtin_bit_cast(u32x4, q0);
#pragma unroll
    for (int i = 0; i < 4; ++i) { float x = bflo(a[i]), y = bfhi(a[i]); qn2 = __builtin_fmaf(x, x, qn2); qn2 = __builtin_fmaf(y, y, qn2);
      x = bflo(q1[i]); y = bfhi(q1[i]); qn2 = __builtin_fmaf(x, x, qn2); qn2 = __builtin_fmaf(y, y, qn2); } }
  qn2 += __shfl_xor(qn2, 32, 64);
#pragma unroll
  for (int o = 32; o > 0; o >>= 1) kn2 = __builtin_fmaxf(kn2, __shfl_xor(kn2, o, 64));
  const float bound = __builtin_sqrtf(qn2 * kn2) * 1.01f + 1.0f;

  f32x16 o = f32x16{};
  const lds_cptr rd0 = (lds_cptr)lds + kh * 8192 + lane * 16;
  float mref, m0;
  auto set_ref = [&](float m) {
    const float mh = bf_round(m), ml = bf_round(m - mh); mref = mh + ml;
    const unsigned pk = cvtpk(-mh, -ml); if (hi) q1[2] = pk; };
#define Q1 __builtin_bit_cast(bf16x8, q1)
#define EXPK(S_, i) cvtpk(__builtin_amdgcn_exp2f(S_[i]), __builtin_amdgcn_exp2f(S_[i + 1]))

  lbar();
  bf16x8 ka0 = lds_rd128(rd0), ka1 = lds_rd128(rd0 + 1024);
  { f32x16 s = MFMA(ka0, q0, f32x16{}); s = MFMA(ka1, Q1, s); m0 = xhalf_max(max16(s)); }
  const bool fast = __all(bound - m0 <= 198.f);
  set_ref(fast ? __builtin_fmaxf(m0, bound - 108.f) : m0);

  if (fast) {
    f32x16 sc, sn; bf16x8 kb0, kb1, va0, va1, vb0, vb1;
    sc = MFMA(ka0, q0, f32x16{}); sc = MFMA(ka1, Q1, sc);
    ka0 = lds_rd128(rd0 + 4096); ka1 = lds_rd128(rd0 + 4096 + 1024); va0 = lds_rd128(rd0 + 2048); va1 = lds_rd128(rd0 + 3072);
#define ITER(SC_, SN_, K0, K1, V0, V1, NK0, NK1, NV0, NV1, PK_, PV_, HASK, HASV, HASN) do { \
      if (HASK) { NK0 = lds_rd128(PK_); NK1 = lds_rd128((PK_) + 1024); } \
      if (HASV) { NV0 = lds_rd128((PV_) + 2048); NV1 = lds_rd128((PV_) + 3072); } SB(); \
      u32x4 p0, p1; \
      p0[0] = EXPK(SC_, 0); p0[1] = EXPK(SC_, 2); SB(); \
      if (HASN) SN_ = MFMA(K0, q0, f32x16{}); SB(); \
      p0[2] = EXPK(SC_, 4); p0[3] = EXPK(SC_, 6); SB(); \
      o = MFMA(V0, __builtin_bit_cast(bf16x8, p0), o); SB(); \
      p1[0] = EXPK(SC_, 8); p1[1] = EXPK(SC_, 10); SB(); \
      if (HASN) SN_ = MFMA(K1, Q1, SN_); SB(); \
      p1[2] = EXPK(SC_, 12); p1[3] = EXPK(SC_, 14); SB(); \
      o = MFMA(V1, __builtin_bit_cast(bf16x8, p1), o); SB(); } while (0)
    for (int s = 0; s < 15; ++s) {
      const lds_cptr p = rd0 + (s & 3) * A_STAGE, pn = rd0 + ((s + 1) & 3) * A_STAGE;
      if (s > 0) lbar();
      ITER(sc, sn, ka0, ka1, va0, va1, kb0, kb1, vb0, vb1, pn, p + 4096, true, true, true);
      ITER(sn, sc, kb0, kb1, vb0, vb1, ka0, ka1, va0, va1, pn + 4096, pn, true, true, true);
    }
    { const lds_cptr p = rd0 + 3 * A_STAGE;
      ITER(sc, sn, ka0, ka1, va0, va1, kb0, kb1, vb0, vb1, p, p + 4096, false, true, true);
      ITER(sn, sc, kb0, kb1, vb0, vb1, ka0, ka1, va0, va1, p, p, false, false, false); }
  } else {
    auto tile = [&](lds_cptr p) {
      const bf16x8 kf0 = lds_rd128(p), kf1 = lds_rd128(p + 1024), vf0 = lds_rd128(p + 2048), vf1 = lds_rd128(p + 3072);
      f32x16 s = MFMA(kf0, q0, f32x16{}); s = MFMA(kf1, Q1, s);
      const float tm = max16(s);
      if (__any(tm > THR)) {
        const float old = mref; set_ref(mref + __builtin_fmaxf(xhalf_max(tm), 0.f)); const float dl = mref - old; const float al = __builtin_amdgcn_exp2f(-dl);
#pragma unroll
        for (int r = 0; r < 16; ++r) { s[r] -= dl; o[r] *= al; }
      }
      u32x4 p0, p1;
#pragma unroll
      for (int i = 0; i < 4; ++i) { p0[i] = EXPK(s, 2 * i); p1[i] = EXPK(s, 8 + 2 * i); }
      o = MFMA(vf0, __builtin_bit_cast(bf16x8, p0), o); o = MFMA(vf1, __builtin_bit_cast(bf16x8, p1), o);
    };
    for (int s = 0; s < 16; ++s) {
      const lds_cptr p = rd0 + (s & 3) * A_STAGE;
      if (s > 0 && s < 15) lbar();
      tile(p); tile(p + 4096);
    }
  }

  float* mrg = (float*)(lds + A_LDS_MRG) + qg * 17 * 64;
  if (kh == 1) {
#pragma unroll
    for (int r = 0; r < 16; ++r) mrg[r * 64 + lane] = o[r];
    mrg[16 * 64 + lane] = -mref;
  }
  lbar();
  if (kh == 0) {
    const float nb = mrg[16 * 64 + lane], na = -mref;
    const float nm = __builtin_fminf(na, nb);
    const float fa = __builtin_amdgcn_exp2f(nm - na), fb = __builtin_amdgcn_exp2f(nm - nb);
#pragma unroll
    for (int r = 0; r < 16; ++r) o[r] = o[r] * fa + mrg[r * 64 + lane] * fb;
    const float l = __shfl(o[12], r32 + 32, 64);
    const float il = 1.0f / l;
    u16* dst = O + (size_t)(row0 + r32) * 896 + head * 28 + 4 * hi;
#pragma unroll
    for (int g = 0; g < 4; ++g) if (!(g == 3 && hi == 1))
      *(u32x2*)(dst + 8 * g) = u32x2{cvtpk(o[4 * g] * il, o[4 * g + 1] * il), cvtpk(o[4 * g + 2] * il, o[4 * g + 3] * il)};
  } else {
    const int t = tid - 256, pr = t >> 1, hf = t & 1, rl = pr >> 4, hb = pr & 15; const int r = brow0 + rl;
    const char* img = lds + A_LDS_SIDE + hb * 56 + hf * 28;
    const unsigned* qv = (const unsigned*)(img + (r - bstart) * 2688);
    unsigned qu[7];
#pragma unroll
    for (int i = 0; i < 7; ++i) qu[i] = qv[i];
    float lg[8]; float m = -3.0e38f;
#pragma unroll
    for (int w = 0; w < 8; ++w) { const int jr = r - 2 * w; const int jc = jr >= 0 ? jr : bstart;
      const unsigned* kp = (const unsigned*)(img + (jc - bstart) * 2688 + 896); float a = 0.f;
#pragma unroll
      for (int i = 0; i < 7; ++i) a = dot2(qu[i], kp[i], a);
      a += __uint_as_float(__builtin_amdgcn_update_dpp(0, __float_as_uint(a), 0xB1, 0xF, 0xF, true));
      lg[w] = jr >= 0 ? a * SCALE : -1e9f; m = __builtin_fmaxf(m, lg[w]); }
    float l = 0.f;
#pragma unroll
    for (int w = 0; w < 8; ++w) { lg[w] = __builtin_amdgcn_exp2f((lg[w] - m) * LOG2E); l += lg[w]; }
    const float il = 1.0f / l; float oa[14];
#pragma unroll
    for (int i = 0; i < 14; ++i) oa[i] = 0.f;
#pragma unroll
    for (int w = 0; w < 8; w += 2) { const int j0 = r - 2 * w, j1 = j0 - 2; const int c0 = j0 >= 0 ? j0 : bstart, c1 = j1 >= 0 ? j1 : bstart;
      const unsigned pp = cvtpk(lg[w] * il, lg[w + 1] * il);
      const unsigned* v0 = (const unsigned*)(img + (c0 - bstart) * 2688 + 1792); const unsigned* v1 = (const unsigned*)(img + (c1 - bstart) * 2688 + 1792);
#pragma unroll
      for (int i = 0; i < 7; ++i) { const unsigned a = v0[i], bb = v1[i];
        oa[2 * i] = dot2(pp, __builtin_amdgcn_perm(bb, a, 0x05040100), oa[2 * i]); oa[2 * i + 1] = dot2(pp, __builtin_amdgcn_perm(bb, a, 0x07060302), oa[2 * i + 1]); } }
    unsigned* dst = (unsigned*)(O + (size_t)r * 896 + 448 + hb * 28 + hf * 14);
#pragma unroll
    for (int i = 0; i < 7; ++i) dst[i] = cvtpk(oa[2 * i], oa[2 * i + 1]);
  }
}

extern "C" void kernel_launch(void* const* d_in, const int* in_sizes, int n_in, void* d_out, int out_size, void* d_ws, size_t ws_size, hipStream_t stream) {
  const u16 *Ax = (const u16*)d_in[0], *Awq = (const u16*)d_in[1], *Awk = (const u16*)d_in[2], *Awv = (const u16*)d_in[3], *Awo = (const u16*)d_in[4];
  const u16 *Bx = (const u16*)d_in[5], *Bw = (const u16*)d_in[6], *Bwo = (const u16*)d_in[7];
  char* ws = (char*)d_ws;
  (void)hipFuncSetAttribute((const void*)k1_proj, hipFuncAttributeMaxDynamicSharedMemorySize, K1_LDS);
  (void)hipFuncSetAttribute((const void*)k2_attn, hipFuncAttributeMaxDynamicSharedMemorySize, A_LDS);
  (void)hipFuncSetAttribute((const void*)k3_out, hipFuncAttributeMaxDynamicSharedMemorySize, K3_LDS);
  k1_proj<<<256, 512, K1_LDS, stream>>>(Ax, Awq, Awk, Awv, Bx, Bw, ws);
  k2_attn<<<256, 576, A_LDS, stream>>>(ws);
  k3_out<<<224, 512, K3_LDS, stream>>>(ws, Awo, Bwo, (u16*)d_out);
}
```

```cpp
#include <hip/hip_runtime.h>
#include <stdint.h>

typedef unsigned short u16;
using bf16x8 = __attribute__((ext_vector_type(8))) short;
using s16x4  = __attribute__((ext_vector_type(4))) short;
using f32x16 = __attribute__((ext_vector_type(16))) float;
using u32x4  = __attribute__((ext_vector_type(4))) unsigned;
using u32x2  = __attribute__((ext_vector_type(2))) unsigned;
typedef __attribute__((address_space(3))) const char* lds_cptr;
typedef __attribute__((address_space(3))) char* lds_ptr;
typedef short v4i16_t __attribute__((ext_vector_type(4)));
typedef __bf16 bf16x2_t __attribute__((ext_vector_type(2)));
typedef float f32x2_t __attribute__((ext_vector_type(2)));
#define DI __device__ __forceinline__
#define MFMA(a, b, c) __builtin_amdgcn_mfma_f32_32x32x16_bf16((a), (b), (c), 0, 0, 0)
#define SB() __builtin_amdgcn_sched_barrier(0)

constexpr int S = 2048, DM = 448, NH = 16, DH = 28;
constexpr float SCALE = 0.18898223650461362f;
constexpr float LOG2E = 1.4426950408889634f;
constexpr float QSCALE = SCALE * LOG2E;

constexpr size_t WS_Q   = 0;
constexpr size_t WS_KV  = WS_Q + (size_t)NH * S * 32 * 2;
constexpr size_t WS_KN  = WS_KV + (size_t)64 * 4096;
constexpr size_t WS_QKV = WS_KN + 256;
constexpr size_t WS_O   = WS_QKV + (size_t)S * 1344 * 2 + 8192;

DI unsigned cvtpk(float lo, float hi) { f32x2_t v = {lo, hi}; return __builtin_bit_cast(unsigned, __builtin_convertvector(v, bf16x2_t)); }
DI void glds16(const void* g, unsigned lds_base) {
  unsigned sv; asm volatile("s_mov_b32 %0, m0\n\ts_mov_b32 m0, %2\n\ts_nop 0\n\tglobal_load_lds_dwordx4 %1, off\n\ts_mov_b32 m0, %0"
                            : "=&s"(sv) : "v"(g), "s"(lds_base) : "memory"); }
template <int N> DI void wait_bar() { asm volatile("s_waitcnt vmcnt(%0) lgkmcnt(0)\n\ts_barrier" :: "i"(N) : "memory"); }
DI bf16x8 lds_rd128(lds_cptr p) { return *(const __attribute__((address_space(3))) bf16x8*)p; }
DI s16x4 lds_tr(lds_cptr p) { return __builtin_bit_cast(s16x4, __builtin_amdgcn_ds_read_tr16_b64_v4i16((__attribute__((address_space(3))) v4i16_t*)p)); }
DI unsigned sfl(unsigned x) { return (unsigned)__builtin_amdgcn_readfirstlane((int)x); }
template <int I> struct IC { static constexpr int value = I; };
template <int I, int N, class F> DI void static_for(F&& f) { if constexpr (I < N) { f(IC<I>{}); static_for<I + 1, N>(f); } }
DI float bflo(unsigned u) { return __uint_as_float(u << 16); }
DI float bfhi(unsigned u) { return __uint_as_float(u & 0xffff0000u); }

constexpr int G_NBUF = 4;
template <int NPP> struct GemmCfg { static constexpr int STAGE_BYTES = 16384 + NPP * 8192; static constexpr int RING = STAGE_BYTES * G_NBUF; };
constexpr int K1_LDS = GemmCfg<2>::RING;
static_assert(GemmCfg<0>::RING + 2 * 448 * 64 <= K1_LDS, "");
constexpr int K3_LDS = GemmCfg<1>::RING;

template <int MODE, int NPP>
DI void gemm_tile(char* lds, const u16* __restrict__ X, int ldx, int row0, const u16* __restrict__ W, int ldw, int col0, int ncols,
                  const u16* __restrict__ Wk, const u16* __restrict__ Wv, u16* __restrict__ out0, u16* __restrict__ out1) {
  constexpr int SBY = GemmCfg<NPP>::STAGE_BYTES;
  constexpr int NPW = (MODE == 1) ? 1 : NPP;
  constexpr int L = 2 + NPP;
  const int tid = threadIdx.x, lane = tid & 63, r32 = lane & 31, hi = lane >> 5;
  const int wid = (int)sfl(tid >> 6);
  const unsigned lds0 = (unsigned)(uintptr_t)lds;
  const u16* xsrc[2];
#pragma unroll
  for (int i = 0; i < 2; ++i) { const int row = (2 * wid + i) * 8 + (lane >> 3); const int c = (lane & 7) ^ ((row >> 1) & 7);
    xsrc[i] = X + (size_t)(row0 + row) * ldx + c * 8; }
  const u16* wsrc[NPP > 0 ? NPP : 1];
#pragma unroll
  for (int i = 0; i < NPP; ++i) { int pc = col0 + i * 64; if (pc > ncols - 64) pc = ncols - 64;
    const int k = wid * 8 + (lane >> 3); const int c = (lane & 7) ^ (((k >> 1) & 1) << 2);
    wsrc[i] = W + (size_t)k * ldw + pc + c * 8; }

  auto issue = [&](int s) {
    const unsigned base = lds0 + (unsigned)((s % G_NBUF) * SBY);
    glds16(xsrc[0] + s * 64, sfl(base + (2 * wid) * 1024));
    glds16(xsrc[1] + s * 64, sfl(base + (2 * wid + 1) * 1024));
#pragma unroll
    for (int i = 0; i < NPP; ++i) glds16(wsrc[i] + (size_t)s * 64 * ldw, sfl(base + 16384 + i * 8192 + wid * 1024));
  };

  const int wr = wid & 3, wc = wid >> 2;
  const int xrow = wr * 32 + r32; const int xsw = (xrow >> 1) & 7;
  const int g16 = lane >> 4, i16 = lane & 15, tq = i16 >> 2;
  const int toff128 = (8 * (g16 >> 1) + tq) * 128 + (g16 & 1) * 32 + (i16 & 3) * 8; const int tsw = (tq >> 1) & 1;
  const int toff64  = (8 * (g16 >> 1) + tq) * 64 + (g16 & 1) * 32 + (i16 & 3) * 8;

  f32x16 acc[NPW];
#pragma unroll
  for (int p = 0; p < NPW; ++p) acc[p] = f32x16{};

  u32x2 kvreg[13];
  if constexpr (MODE == 1) {
#pragma unroll
    for (int i = 0; i < 13; ++i) { int e = tid + i * 512; if (e > 6271) e = 6271; const int m = e >= 3136; const int e2 = e - m * 3136;
      kvreg[i] = *(const u32x2*)((m ? Wv : Wk) + e2 * 4); }
  }
  issue(0); issue(1); issue(2);
  if constexpr (MODE == 1) {
#pragma unroll
    for (int i = 0; i < 13; ++i) { int e = tid + i * 512; if (e > 6271) e = 6271; const int m = e >= 3136; const int e2 = e - m * 3136; const int k = e2 / 7, c = e2 - k * 7;
      *(u32x2*)(lds + GemmCfg<0>::RING + m * 28672 + k * 64 + c * 8) = kvreg[i]; }
  }
  static_for<0, 7>([&](auto sc) {
    constexpr int s = decltype(sc)::value;
    constexpr int ahead = (s + 2 < 6 ? s + 2 : 6) - s;
    wait_bar<ahead * L>();
    if constexpr (s + 3 <= 6) issue(s + 3);
    const lds_cptr b = (lds_cptr)lds + (s % G_NBUF) * SBY;
#pragma unroll
    for (int ks = 0; ks < 4; ++ks) {
      const bf16x8 xf = lds_rd128(b + xrow * 128 + (((2 * ks + hi) ^ xsw) * 16));
#pragma unroll
      for (int p = 0; p < NPW; ++p) {
        lds_cptr a; int step;
        if constexpr (MODE == 1) { a = (lds_cptr)lds + GemmCfg<0>::RING + wc * 28672 + (s * 64 + ks * 16) * 64 + toff64; step = 256; }
        else if constexpr (NPP == 2) { a = b + 16384 + wc * 8192 + ks * 2048 + toff128 + ((p ^ tsw) * 64); step = 512; }
        else { a = b + 16384 + ks * 2048 + toff128 + ((wc ^ tsw) * 64); step = 512; }
        const s16x4 lo = lds_tr(a), hh = lds_tr(a + step);
        const bf16x8 wf = __builtin_shufflevector(lo, hh, 0, 1, 2, 3, 4, 5, 6, 7);
        if constexpr (MODE == 1) { const bf16x8 fa = wc ? xf : wf, fb = wc ? wf : xf; acc[p] = MFMA(fa, fb, acc[p]); }
        else acc[p] = MFMA(wf, xf, acc[p]);
      }
    }
  });

  const int row = row0 + xrow;
  if constexpr (MODE >= 2) {
    constexpr int RB = NPW * 64 + 16;
    __syncthreads();
    char* st = lds + wid * (32 * RB);
#pragma unroll
    for (int p = 0; p < NPW; ++p)
#pragma unroll
      for (int g = 0; g < 4; ++g)
        *(u32x2*)(st + r32 * RB + p * 64 + g * 16 + hi * 8) = u32x2{cvtpk(acc[p][4 * g], acc[p][4 * g + 1]), cvtpk(acc[p][4 * g + 2], acc[p][4 * g + 3])};
    asm volatile("s_waitcnt lgkmcnt(0)" ::: "memory");
    constexpr int LPR = NPW * 4;
    const int ld = MODE == 2 ? 1344 : 896;
    const int cbase = col0 + wc * NPW * 32;
    if (MODE == 3 || cbase < 1344) {
#pragma unroll
      for (int i = 0; i < 32 * LPR / 64; ++i) { const int rr = i * (64 / LPR) + lane / LPR, ch = lane % LPR;
        *(u32x4*)(out0 + (size_t)(row0 + wr * 32 + rr) * ld + cbase + ch * 8) = *(const u32x4*)(st + rr * RB + ch * 16); }
    }
  } else {
#pragma unroll
    for (int p = 0; p < NPW; ++p) {
      const int panel = wc * NPW + p;
      if (MODE == 1) {
        const int tile = (row0 >> 5) + wr; u16* kv = out1 + (size_t)tile * 2048;
        if (wc == 0) {
          float ss = 0.f;
#pragma unroll
          for (int g = 0; g < 4; ++g) { u32x2 v;
            if (g == 3 && hi == 1) v = u32x2{0x3F803F80u, 0u};
            else { v = u32x2{cvtpk(acc[p][4 * g], acc[p][4 * g + 1]), cvtpk(acc[p][4 * g + 2], acc[p][4 * g + 3])};
#pragma unroll
              for (int i = 0; i < 2; ++i) { const float a = bflo(v[i]), b = bfhi(v[i]); ss = __builtin_fmaf(a, a, ss); ss = __builtin_fmaf(b, b, ss); } }
            *(u32x2*)(kv + (g >> 1) * 512 + (r32 + 32 * (g & 1)) * 8 + 4 * hi) = v; }
          ss += __shfl_xor(ss, 32, 64);
#pragma unroll
          for (int o = 16; o > 0; o >>= 1) ss = __builtin_fmaxf(ss, __shfl_xor(ss, o, 64));
          if (lane == 0) ((float*)((char*)out1 + (WS_KN - WS_KV)))[tile] = ss;
        } else {
#pragma unroll
          for (int g = 0; g < 4; ++g) { u32x2 v = u32x2{cvtpk(acc[p][4 * g], acc[p][4 * g + 1]), cvtpk(acc[p][4 * g + 2], acc[p][4 * g + 3])};
            if (r32 == 28) v = u32x2{0x3F803F80u, 0x3F803F80u}; else if (r32 > 28) v = u32x2{0u, 0u};
            *(u32x2*)(kv + 1024 + (g >> 1) * 512 + lane * 8 + 4 * (g & 1)) = v; }
        }
      } else {
#pragma unroll
        for (int g = 0; g < 4; ++g) { const int c = col0 + panel * 32 + 8 * g + 4 * hi; const int head = c / 28, d = c - head * 28;
          u32x2 v = u32x2{cvtpk(acc[p][4 * g] * QSCALE, acc[p][4 * g + 1] * QSCALE), cvtpk(acc[p][4 * g + 2] * QSCALE, acc[p][4 * g + 3] * QSCALE)};
          u16* dst = out0 + ((size_t)head * S + row) * 32 + d;
          if (d == 24) *(u32x4*)dst = u32x4{v[0], v[1], 0u, 0u}; else *(u32x2*)dst = v; }
      }
    }
  }
}

__global__ __launch_bounds__(512, 2) void k1_proj(const u16* __restrict__ Ax, const u16* __restrict__ Awq, const u16* __restrict__ Awk, const u16* __restrict__ Awv,
                                                  const u16* __restrict__ Bx, const u16* __restrict__ Bw, char* __restrict__ ws) {
  extern __shared__ __attribute__((aligned(16))) char lds[];
  const int b = blockIdx.x, xcd = b & 7, j = b >> 3; const int rt = 2 * xcd + (j & 1), ct = j >> 1; const int row0 = rt * 128;
  u16* Qs = (u16*)(ws + WS_Q); u16* KV = (u16*)(ws + WS_KV); u16* QKV = (u16*)(ws + WS_QKV);
  if (ct >= 5) gemm_tile<2, 2>(lds, Bx, DM, row0, Bw, 1344, (ct - 5) * 128, 1344, nullptr, nullptr, QKV, nullptr);
  else if (ct >= 2) gemm_tile<0, 2>(lds, Ax, DM, row0, Awq, DM, (ct - 2) * 128, DM, nullptr, nullptr, Qs, nullptr);
  else if (ct == 1) gemm_tile<0, 1>(lds, Ax, DM, row0, Awq, DM, 384, DM, nullptr, nullptr, Qs, nullptr);
  else gemm_tile<1, 0>(lds, Ax, DM, row0, Awq, DM, 0, DM, Awk, Awv, Qs, KV);
}

__global__ __launch_bounds__(512, 2) void k3_out(const char* __restrict__ ws, const u16* __restrict__ Awo, const u16* __restrict__ Bwo, u16* __restrict__ out) {
  extern __shared__ __attribute__((aligned(16))) char lds[];
  const int b = blockIdx.x, xcd = b & 7, j = b >> 3; const int rt = 2 * xcd + (j & 1), ct = j >> 1; const int row0 = rt * 128;
  const u16* O = (const u16*)(ws + WS_O);
  const int part = ct >= 7 ? 1 : 0; const int c0 = (ct - 7 * part) * 64;
  gemm_tile<3, 1>(lds, O + part * 448, 896, row0, part ? Bwo : Awo, DM, c0, DM, nullptr, nullptr, out + part * 448, nullptr);
}

constexpr int A_STAGE = 16384;
constexpr int A_LDS_SIDE = 4 * A_STAGE;
constexpr int A_LDS_MRG = A_LDS_SIDE + 65536;
constexpr int A_LDS = A_LDS_MRG + 4 * 17 * 256;
constexpr float THR = 30.f;

DI float max16(const f32x16& s) {
  float a = __builtin_fmaxf(__builtin_fmaxf(s[0], s[1]), s[2]), b = __builtin_fmaxf(__builtin_fmaxf(s[3], s[4]), s[5]);
  a = __builtin_fmaxf(__builtin_fmaxf(a, s[6]), s[7]); b = __builtin_fmaxf(__builtin_fmaxf(b, s[8]), s[9]);
  a = __builtin_fmaxf(__builtin_fmaxf(a, s[10]), s[11]); b = __builtin_fmaxf(__builtin_fmaxf(b, s[12]), s[13]);
  a = __builtin_fmaxf(__builtin_fmaxf(a, s[14]), s[15]);
  return __builtin_fmaxf(a, b); }
DI float xhalf_max(float m) { auto rr = __builtin_amdgcn_permlane32_swap(__float_as_uint(m), __float_as_uint(m), false, false);
  return __builtin_fmaxf(__uint_as_float(rr[0]), __uint_as_float(rr[1])); }
DI float bf_round(float x) { return __uint_as_float(cvtpk(x, 0.f) << 16); }

DI float dot2(unsigned a, unsigned b, float c) { return __builtin_amdgcn_fdot2_f32_bf16(__builtin_bit_cast(bf16x2_t, a), __builtin_bit_cast(bf16x2_t, b), c, false); }
DI void lbar() { asm volatile("s_waitcnt lgkmcnt(0)\n\ts_barrier" ::: "memory"); }

__global__ __launch_bounds__(768, 1) void k2_attn(char* __restrict__ ws) {
  extern __shared__ __attribute__((aligned(16))) char lds[];
  const int tid = threadIdx.x, lane = tid & 63, r32 = lane & 31, hi = lane >> 5;
  const int wid = (int)sfl(tid >> 6), qg = wid & 3, kh = (wid >> 2) & 1;
  const int b = blockIdx.x, xcd = b & 7, j = b >> 3;
  const int rc = xcd * 8 + (j >> 2), hg = j & 3;
  const int head = hg * 4 + qg, row0 = rc * 32;
  const u16* Qs = (const u16*)(ws + WS_Q); const char* KV = ws + WS_KV; const char* QKV = ws + WS_QKV; u16* O = (u16*)(ws + WS_O);
  const unsigned lds0 = (unsigned)(uintptr_t)lds;
  const int brow0 = (xcd * 32 + j) * 8; const int bstart = brow0 >= 14 ? brow0 - 14 : 0;

  if (wid >= 8) {
    const char* kvsrc = KV + (size_t)(qg >> 1) * 131072 + (qg & 1) * 4096 + lane * 16;
    const unsigned kvdst = lds0 + qg * 4096;
    const char* sdsrc = QKV + (size_t)bstart * 2688 + qg * 2048 + lane * 16;
    auto issue_kv = [&](int s) { const unsigned d = kvdst + (s & 3) * A_STAGE;
#pragma unroll
      for (int i = 0; i < 4; ++i) glds16(kvsrc + (size_t)s * 8192 + i * 1024, d + i * 1024); };
    issue_kv(0); issue_kv(1); issue_kv(2); issue_kv(3);
    wait_bar<8>();
    for (int s = 1; s < 15; ++s) {
      if (s < 14) wait_bar<4>(); else wait_bar<0>();
      if (s <= 8) {
#pragma unroll
        for (int i = 0; i < 2; ++i) glds16(sdsrc + (s - 1) * 8192 + i * 1024, lds0 + A_LDS_SIDE + (s - 1) * 8192 + qg * 2048 + i * 1024); }
      if (s <= 12) issue_kv(s + 3);
    }
    {
    const int t = tid - 512, pr = t >> 1, hf = t & 1, rl = pr >> 4, hb = pr & 15; const int r = brow0 + rl;
    const char* img = lds + A_LDS_SIDE + hb * 56 + hf * 28;
    const unsigned* qv = (const unsigned*)(img + (r - bstart) * 2688);
    unsigned qu[7];
#pragma unroll
    for (int i = 0; i < 7; ++i) qu[i] = qv[i];
    float lg[8]; float m = -3.0e38f;
#pragma unroll
    for (int w = 0; w < 8; ++w) { const int jr = r - 2 * w; const int jc = jr >= 0 ? jr : bstart;
      const unsigned* kp = (const unsigned*)(img + (jc - bstart) * 2688 + 896); float a = 0.f;
#pragma unroll
      for (int i = 0; i < 7; ++i) a = dot2(qu[i], kp[i], a);
      a += __uint_as_float(__builtin_amdgcn_update_dpp(0, __float_as_uint(a), 0xB1, 0xF, 0xF, true));
      lg[w] = jr >= 0 ? a * SCALE : -1e9f; m = __builtin_fmaxf(m, lg[w]); }
    float l = 0.f;
#pragma unroll
    for (int w = 0; w < 8; ++w) { lg[w] = __builtin_amdgcn_exp2f((lg[w] - m) * LOG2E); l += lg[w]; }
    const float il = 1.0f / l; float oa[14];
#pragma unroll
    for (int i = 0; i < 14; ++i) oa[i] = 0.f;
#pragma unroll
    for (int w = 0; w < 8; w += 2) { const int j0 = r - 2 * w, j1 = j0 - 2; const int c0 = j0 >= 0 ? j0 : bstart, c1 = j1 >= 0 ? j1 : bstart;
      const unsigned pp = cvtpk(lg[w] * il, lg[w + 1] * il);
      const unsigned* v0 = (const unsigned*)(img + (c0 - bstart) * 2688 + 1792); const unsigned* v1 = (const unsigned*)(img + (c1 - bstart) * 2688 + 1792);
#pragma unroll
      for (int i = 0; i < 7; ++i) { const unsigned a = v0[i], bb = v1[i];
        oa[2 * i] = dot2(pp, __builtin_amdgcn_perm(bb, a, 0x05040100), oa[2 * i]); oa[2 * i + 1] = dot2(pp, __builtin_amdgcn_perm(bb, a, 0x07060302), oa[2 * i + 1]); } }
    unsigned* dst = (unsigned*)(O + (size_t)r * 896 + 448 + hb * 28 + hf * 14);
#pragma unroll
    for (int i = 0; i < 7; ++i) dst[i] = cvtpk(oa[2 * i], oa[2 * i + 1]);
    }
    lbar();
    return;
  }

  const u16* qp = Qs + ((size_t)head * S + row0 + r32) * 32 + hi * 8;
  const bf16x8 q0 = *(const bf16x8*)qp; u32x4 q1 = *(const u32x4*)(qp + 16);
  float kn2 = ((const float*)(ws + WS_KN))[lane];

  float qn2 = 0.f;
  { const u32x4 a = __builtin_bit_cast(u32x4, q0);
#pragma unroll
    for (int i = 0; i < 4; ++i) { float x = bflo(a[i]), y = bfhi(a[i]); qn2 = __builtin_fmaf(x, x, qn2); qn2 = __builtin_fmaf(y, y, qn2);
      x = bflo(q1[i]); y = bfhi(q1[i]); qn2 = __builtin_fmaf(x, x, qn2); qn2 = __builtin_fmaf(y, y, qn2); } }
  qn2 += __shfl_xor(qn2, 32, 64);
#pragma unroll
  for (int o = 32; o > 0; o >>= 1) kn2 = __builtin_fmaxf(kn2, __shfl_xor(kn2, o, 64));
  const float bound = __builtin_sqrtf(qn2 * kn2) * 1.01f + 1.0f;

  f32x16 o = f32x16{};
  const lds_cptr rd0 = (lds_cptr)lds + kh * 8192 + lane * 16;
  float mref, m0;
  auto set_ref = [&](float m) {
    const float mh = bf_round(m), ml = bf_round(m - mh); mref = mh + ml;
    const unsigned pk = cvtpk(-mh, -ml); if (hi) q1[2] = pk; };
#define Q1 __builtin_bit_cast(bf16x8, q1)
#define EXPK(S_, i) cvtpk(__builtin_amdgcn_exp2f(S_[i]), __builtin_amdgcn_exp2f(S_[i + 1]))

  lbar();
  bf16x8 ka0 = lds_rd128(rd0), ka1 = lds_rd128(rd0 + 1024);
  { f32x16 s = MFMA(ka0, q0, f32x16{}); s = MFMA(ka1, Q1, s); m0 = xhalf_max(max16(s)); }
  const bool fast = __all(bound - m0 <= 198.f);
  set_ref(fast ? __builtin_fmaxf(m0, bound - 108.f) : m0);

  if (fast) {
    f32x16 sc, sn; bf16x8 kb0, kb1, va0, va1, vb0, vb1;
    sc = MFMA(ka0, q0, f32x16{}); sc = MFMA(ka1, Q1, sc);
    ka0 = lds_rd128(rd0 + 4096); ka1 = lds_rd128(rd0 + 4096 + 1024); va0 = lds_rd128(rd0 + 2048); va1 = lds_rd128(rd0 + 3072);
#define ITER(SC_, SN_, K0, K1, V0, V1, NK0, NK1, NV0, NV1, PK_, PV_, HASK, HASV, HASN) do { \
      if (HASK) { NK0 = lds_rd128(PK_); NK1 = lds_rd128((PK_) + 1024); } \
      if (HASV) { NV0 = lds_rd128((PV_) + 2048); NV1 = lds_rd128((PV_) + 3072); } SB(); \
      u32x4 p0, p1; \
      p0[0] = EXPK(SC_, 0); p0[1] = EXPK(SC_, 2); SB(); \
      if (HASN) SN_ = MFMA(K0, q0, f32x16{}); SB(); \
      p0[2] = EXPK(SC_, 4); p0[3] = EXPK(SC_, 6); SB(); \
      o = MFMA(V0, __builtin_bit_cast(bf16x8, p0), o); SB(); \
      p1[0] = EXPK(SC_, 8); p1[1] = EXPK(SC_, 10); SB(); \
      if (HASN) SN_ = MFMA(K1, Q1, SN_); SB(); \
      p1[2] = EXPK(SC_, 12); p1[3] = EXPK(SC_, 14); SB(); \
      o = MFMA(V1, __builtin_bit_cast(bf16x8, p1), o); SB(); } while (0)
    for (int s = 0; s < 15; ++s) {
      const lds_cptr p = rd0 + (s & 3) * A_STAGE, pn = rd0 + ((s + 1) & 3) * A_STAGE;
      if (s > 0) lbar();
      ITER(sc, sn, ka0, ka1, va0, va1, kb0, kb1, vb0, vb1, pn, p + 4096, true, true, true);
      ITER(sn, sc, kb0, kb1, vb0, vb1, ka0, ka1, va0, va1, pn + 4096, pn, true, true, true);
    }
    { const lds_cptr p = rd0 + 3 * A_STAGE;
      ITER(sc, sn, ka0, ka1, va0, va1, kb0, kb1, vb0, vb1, p, p + 4096, false, true, true);
      ITER(sn, sc, kb0, kb1, vb0, vb1, ka0, ka1, va0, va1, p, p, false, false, false); }
  } else {
    auto tile = [&](lds_cptr p) {
      const bf16x8 kf0 = lds_rd128(p), kf1 = lds_rd128(p + 1024), vf0 = lds_rd128(p + 2048), vf1 = lds_rd128(p + 3072);
      f32x16 s = MFMA(kf0, q0, f32x16{}); s = MFMA(kf1, Q1, s);
      const float tm = max16(s);
      if (__any(tm > THR)) {
        const float old = mref; set_ref(mref + __builtin_fmaxf(xhalf_max(tm), 0.f)); const float dl = mref - old; const float al = __builtin_amdgcn_exp2f(-dl);
#pragma unroll
        for (int r = 0; r < 16; ++r) { s[r] -= dl; o[r] *= al; }
      }
      u32x4 p0, p1;
#pragma unroll
      for (int i = 0; i < 4; ++i) { p0[i] = EXPK(s, 2 * i); p1[i] = EXPK(s, 8 + 2 * i); }
      o = MFMA(vf0, __builtin_bit_cast(bf16x8, p0), o); o = MFMA(vf1, __builtin_bit_cast(bf16x8, p1), o);
    };
    for (int s = 0; s < 16; ++s) {
      const lds_cptr p = rd0 + (s & 3) * A_STAGE;
      if (s > 0 && s < 15) lbar();
      tile(p); tile(p + 4096);
    }
  }

  float* mrg = (float*)(lds + A_LDS_MRG) + qg * 17 * 64;
  if (kh == 1) {
#pragma unroll
    for (int r = 0; r < 16; ++r) mrg[r * 64 + lane] = o[r];
    mrg[16 * 64 + lane] = -mref;
  }
  lbar();
  if (kh == 0) {
    const float nb = mrg[16 * 64 + lane], na = -mref;
    const float nm = __builtin_fminf(na, nb);
    const float fa = __builtin_amdgcn_exp2f(nm - na), fb = __builtin_amdgcn_exp2f(nm - nb);
#pragma unroll
    for (int r = 0; r < 16; ++r) o[r] = o[r] * fa + mrg[r * 64 + lane] * fb;
    const float l = __shfl(o[12], r32 + 32, 64);
    const float il = 1.0f / l;
    u16* dst = O + (size_t)(row0 + r32) * 896 + head * 28 + 4 * hi;
#pragma unroll
    for (int g = 0; g < 4; ++g) if (!(g == 3 && hi == 1))
      *(u32x2*)(dst + 8 * g) = u32x2{cvtpk(o[4 * g] * il, o[4 * g + 1] * il), cvtpk(o[4 * g + 2] * il, o[4 * g + 3] * il)};

  }
}

extern "C" void kernel_launch(void* const* d_in, const int* in_sizes, int n_in, void* d_out, int out_size, void* d_ws, size_t ws_size, hipStream_t stream) {
  const u16 *Ax = (const u16*)d_in[0], *Awq = (const u16*)d_in[1], *Awk = (const u16*)d_in[2], *Awv = (const u16*)d_in[3], *Awo = (const u16*)d_in[4];
  const u16 *Bx = (const u16*)d_in[5], *Bw = (const u16*)d_in[6], *Bwo = (const u16*)d_in[7];
  char* ws = (char*)d_ws;
  (void)hipFuncSetAttribute((const void*)k1_proj, hipFuncAttributeMaxDynamicSharedMemorySize, K1_LDS);
  (void)hipFuncSetAttribute((const void*)k2_attn, hipFuncAttributeMaxDynamicSharedMemorySize, A_LDS);
  (void)hipFuncSetAttribute((const void*)k3_out, hipFuncAttributeMaxDynamicSharedMemorySize, K3_LDS);
  k1_proj<<<256, 512, K1_LDS, stream>>>(Ax, Awq, Awk, Awv, Bx, Bw, ws);
  k2_attn<<<256, 768, A_LDS, stream>>>(ws);
  k3_out<<<224, 512, K3_LDS, stream>>>(ws, Awo, Bwo, (u16*)d_out);
}
```

```cpp
#include <hip/hip_runtime.h>
#include <stdint.h>

typedef unsigned short u16;
using bf16x8 = __attribute__((ext_vector_type(8))) short;
using s16x4  = __attribute__((ext_vector_type(4))) short;
using f32x16 = __attribute__((ext_vector_type(16))) float;
using u32x4  = __attribute__((ext_vector_type(4))) unsigned;
using u32x2  = __attribute__((ext_vector_type(2))) unsigned;
typedef __attribute__((address_space(3))) const char* lds_cptr;
typedef __attribute__((address_space(3))) char* lds_ptr;
typedef short v4i16_t __attribute__((ext_vector_type(4)));
typedef __bf16 bf16x2_t __attribute__((ext_vector_type(2)));
typedef float f32x2_t __attribute__((ext_vector_type(2)));
#define DI __device__ __forceinline__
#define MFMA(a, b, c) __builtin_amdgcn_mfma_f32_32x32x16_bf16((a), (b), (c), 0, 0, 0)
#define SB() __builtin_amdgcn_sched_barrier(0)

constexpr int S = 2048, DM = 448, NH = 16, DH = 28;
constexpr float SCALE = 0.18898223650461362f;
constexpr float LOG2E = 1.4426950408889634f;
constexpr float QSCALE = SCALE * LOG2E;

constexpr size_t WS_Q   = 0;
constexpr size_t WS_KV  = WS_Q + (size_t)NH * S * 32 * 2;
constexpr size_t WS_KN  = WS_KV + (size_t)64 * 4096;
constexpr size_t WS_QKV = WS_KN + 256;
constexpr size_t WS_O   = WS_QKV + (size_t)S * 1344 * 2 + 8192;

DI unsigned cvtpk(float lo, float hi) { f32x2_t v = {lo, hi}; return __builtin_bit_cast(unsigned, __builtin_convertvector(v, bf16x2_t)); }
DI void glds16(const void* g, unsigned lds_base) {
  unsigned sv; asm volatile("s_mov_b32 %0, m0\n\ts_mov_b32 m0, %2\n\ts_nop 0\n\tglobal_load_lds_dwordx4 %1, off\n\ts_mov_b32 m0, %0"
                            : "=&s"(sv) : "v"(g), "s"(lds_base) : "memory"); }
template <int N> DI void wait_bar() { asm volatile("s_waitcnt vmcnt(%0) lgkmcnt(0)\n\ts_barrier" :: "i"(N) : "memory"); }
DI bf16x8 lds_rd128(lds_cptr p) { return *(const __attribute__((address_space(3))) bf16x8*)p; }
DI s16x4 lds_tr(lds_cptr p) { return __builtin_bit_cast(s16x4, __builtin_amdgcn_ds_read_tr16_b64_v4i16((__attribute__((address_space(3))) v4i16_t*)p)); }
DI unsigned sfl(unsigned x) { return (unsigned)__builtin_amdgcn_readfirstlane((int)x); }
template <int I> struct IC { static constexpr int value = I; };
template <int I, int N, class F> DI void static_for(F&& f) { if constexpr (I < N) { f(IC<I>{}); static_for<I + 1, N>(f); } }
DI float bflo(unsigned u) { return __uint_as_float(u << 16); }
DI float bfhi(unsigned u) { return __uint_as_float(u & 0xffff0000u); }

constexpr int G_NBUF = 4;
template <int NPP> struct GemmCfg { static constexpr int STAGE_BYTES = 16384 + NPP * 8192; static constexpr int RING = STAGE_BYTES * G_NBUF; };
constexpr int K1_LDS = GemmCfg<2>::RING;
static_assert(GemmCfg<0>::RING + 2 * 448 * 64 <= K1_LDS, "");
constexpr int K3_LDS = GemmCfg<1>::RING;

template <int MODE, int NPP>
DI void gemm_tile(char* lds, const u16* __restrict__ X, int ldx, int row0, const u16* __restrict__ W, int ldw, int col0, int ncols,
                  const u16* __restrict__ Wk, const u16* __restrict__ Wv, u16* __restrict__ out0, u16* __restrict__ out1) {
  constexpr int SBY = GemmCfg<NPP>::STAGE_BYTES;
  constexpr int NPW = (MODE == 1) ? 1 : NPP;
  constexpr int L = 2 + NPP;
  const int tid = threadIdx.x, lane = tid & 63, r32 = lane & 31, hi = lane >> 5;
  const int wid = (int)sfl(tid >> 6);
  const unsigned lds0 = (unsigned)(uintptr_t)lds;
  const u16* xsrc[2];
#pragma unroll
  for (int i = 0; i < 2; ++i) { const int row = (2 * wid + i) * 8 + (lane >> 3); const int c = (lane & 7) ^ ((row >> 1) & 7);
    xsrc[i] = X + (size_t)(row0 + row) * ldx + c * 8; }
  const u16* wsrc[NPP > 0 ? NPP : 1];
#pragma unroll
  for (int i = 0; i < NPP; ++i) { int pc = col0 + i * 64; if (pc > ncols - 64) pc = ncols - 64;
    const int k = wid * 8 + (lane >> 3); const int c = (lane & 7) ^ (((k >> 1) & 1) << 2);
    wsrc[i] = W + (size_t)k * ldw + pc + c * 8; }

  auto issue = [&](int s) {
    const unsigned base = lds0 + (unsigned)((s % G_NBUF) * SBY);
    glds16(xsrc[0] + s * 64, sfl(base + (2 * wid) * 1024));
    glds16(xsrc[1] + s * 64, sfl(base + (2 * wid + 1) * 1024));
#pragma unroll
    for (int i = 0; i < NPP; ++i) glds16(wsrc[i] + (size_t)s * 64 * ldw, sfl(base + 16384 + i * 8192 + wid * 1024));
  };

  const int wr = wid & 3, wc = wid >> 2;
  const int xrow = wr * 32 + r32; const int xsw = (xrow >> 1) & 7;
  const int g16 = lane >> 4, i16 = lane & 15, tq = i16 >> 2;
  const int toff128 = (8 * (g16 >> 1) + tq) * 128 + (g16 & 1) * 32 + (i16 & 3) * 8; const int tsw = (tq >> 1) & 1;
  const int toff64  = (8 * (g16 >> 1) + tq) * 64 + (g16 & 1) * 32 + (i16 & 3) * 8;

  f32x16 acc[NPW];
#pragma unroll
  for (int p = 0; p < NPW; ++p) acc[p] = f32x16{};

  u32x2 kvreg[13];
  if constexpr (MODE == 1) {
#pragma unroll
    for (int i = 0; i < 13; ++i) { int e = tid + i * 512; if (e > 6271) e = 6271; const int m = e >= 3136; const int e2 = e - m * 3136;
      kvreg[i] = *(const u32x2*)((m ? Wv : Wk) + e2 * 4); }
  }
  issue(0); issue(1); issue(2);
  if constexpr (MODE == 1) {
#pragma unroll
    for (int i = 0; i < 13; ++i) { int e = tid + i * 512; if (e > 6271) e = 6271; const int m = e >= 3136; const int e2 = e - m * 3136; const int k = e2 / 7, c = e2 - k * 7;
      *(u32x2*)(lds + GemmCfg<0>::RING + m * 28672 + k * 64 + c * 8) = kvreg[i]; }
  }
  static_for<0, 7>([&](auto sc) {
    constexpr int s = decltype(sc)::value;
    constexpr int ahead = (s + 2 < 6 ? s + 2 : 6) - s;
    wait_bar<ahead * L>();
    if constexpr (s + 3 <= 6) issue(s + 3);
    const lds_cptr b = (lds_cptr)lds + (s % G_NBUF) * SBY;
#pragma unroll
    for (int ks = 0; ks < 4; ++ks) {
      const bf16x8 xf = lds_rd128(b + xrow * 128 + (((2 * ks + hi) ^ xsw) * 16));
#pragma unroll
      for (int p = 0; p < NPW; ++p) {
        lds_cptr a; int step;
        if constexpr (MODE == 1) { a = (lds_cptr)lds + GemmCfg<0>::RING + wc * 28672 + (s * 64 + ks * 16) * 64 + toff64; step = 256; }
        else if constexpr (NPP == 2) { a = b + 16384 + wc * 8192 + ks * 2048 + toff128 + ((p ^ tsw) * 64); step = 512; }
        else { a = b + 16384 + ks * 2048 + toff128 + ((wc ^ tsw) * 64); step = 512; }
        const s16x4 lo = lds_tr(a), hh = lds_tr(a + step);
        const bf16x8 wf = __builtin_shufflevector(lo, hh, 0, 1, 2, 3, 4, 5, 6, 7);
        if constexpr (MODE == 1) { const bf16x8 fa = wc ? xf : wf, fb = wc ? wf : xf; acc[p] = MFMA(fa, fb, acc[p]); }
        else acc[p] = MFMA(wf, xf, acc[p]);
      }
    }
  });

  const int row = row0 + xrow;
  if constexpr (MODE != 1) {
    constexpr int RB = NPW * 64 + 16;
    constexpr float OS = MODE == 0 ? QSCALE : 1.0f;
    __syncthreads();
    char* st = lds + wid * (32 * RB);
#pragma unroll
    for (int p = 0; p < NPW; ++p)
#pragma unroll
      for (int g = 0; g < 4; ++g)
        *(u32x2*)(st + r32 * RB + p * 64 + g * 16 + hi * 8) = u32x2{cvtpk(acc[p][4 * g] * OS, acc[p][4 * g + 1] * OS), cvtpk(acc[p][4 * g + 2] * OS, acc[p][4 * g + 3] * OS)};
    asm volatile("s_waitcnt lgkmcnt(0)" ::: "memory");
    const int cbase = col0 + wc * NPW * 32;
    if constexpr (MODE == 0) {
      constexpr int GPR = NPW * 8;
      const int cg = lane % GPR; const int c = cbase + cg * 4; const int head = c / 28, d = c - head * 28;
#pragma unroll
      for (int i = 0; i < 32 * GPR / 64; ++i) { const int rr = i * (64 / GPR) + lane / GPR;
        const u32x2 v = *(const u32x2*)(st + rr * RB + cg * 8);
        u16* dst = out0 + ((size_t)head * S + row0 + wr * 32 + rr) * 32 + d;
        if (d == 24) *(u32x4*)dst = u32x4{v[0], v[1], 0u, 0u}; else *(u32x2*)dst = v; }
    } else {
      constexpr int LPR = NPW * 4;
      const int ld = MODE == 2 ? 1344 : 896;
      if (MODE == 3 || cbase < 1344) {
#pragma unroll
        for (int i = 0; i < 32 * LPR / 64; ++i) { const int rr = i * (64 / LPR) + lane / LPR, ch = lane % LPR;
          *(u32x4*)(out0 + (size_t)(row0 + wr * 32 + rr) * ld + cbase + ch * 8) = *(const u32x4*)(st + rr * RB + ch * 16); }
      }
    }
  } else {
#pragma unroll
    for (int p = 0; p < NPW; ++p) {
      const int panel = wc * NPW + p;
      if (MODE == 1) {
        const int tile = (row0 >> 5) + wr; u16* kv = out1 + (size_t)tile * 2048;
        if (wc == 0) {
          float ss = 0.f;
#pragma unroll
          for (int g = 0; g < 4; ++g) { u32x2 v;
            if (g == 3 && hi == 1) v = u32x2{0x3F803F80u, 0u};
            else { v = u32x2{cvtpk(acc[p][4 * g], acc[p][4 * g + 1]), cvtpk(acc[p][4 * g + 2], acc[p][4 * g + 3])};
#pragma unroll
              for (int i = 0; i < 2; ++i) { const float a = bflo(v[i]), b = bfhi(v[i]); ss = __builtin_fmaf(a, a, ss); ss = __builtin_fmaf(b, b, ss); } }
            *(u32x2*)(kv + (g >> 1) * 512 + (r32 + 32 * (g & 1)) * 8 + 4 * hi) = v; }
          ss += __shfl_xor(ss, 32, 64);
#pragma unroll
          for (int o = 16; o > 0; o >>= 1) ss = __builtin_fmaxf(ss, __shfl_xor(ss, o, 64));
          if (lane == 0) ((float*)((char*)out1 + (WS_KN - WS_KV)))[tile] = ss;
        } else {
#pragma unroll
          for (int g = 0; g < 4; ++g) { u32x2 v = u32x2{cvtpk(acc[p][4 * g], acc[p][4 * g + 1]), cvtpk(acc[p][4 * g + 2], acc[p][4 * g + 3])};
            if (r32 == 28) v = u32x2{0x3F803F80u, 0x3F803F80u}; else if (r32 > 28) v = u32x2{0u, 0u};
            *(u32x2*)(kv + 1024 + (g >> 1) * 512 + lane * 8 + 4 * (g & 1)) = v; }
        }
      } else {
#pragma unroll
        for (int g = 0; g < 4; ++g) { const int c = col0 + panel * 32 + 8 * g + 4 * hi; const int head = c / 28, d = c - head * 28;
          u32x2 v = u32x2{cvtpk(acc[p][4 * g] * QSCALE, acc[p][4 * g + 1] * QSCALE), cvtpk(acc[p][4 * g + 2] * QSCALE, acc[p][4 * g + 3] * QSCALE)};
          u16* dst = out0 + ((size_t)head * S + row) * 32 + d;
          if (d == 24) *(u32x4*)dst = u32x4{v[0], v[1], 0u, 0u}; else *(u32x2*)dst = v; }
      }
    }
  }
}

__global__ __launch_bounds__(512, 2) void k1_proj(const u16* __restrict__ Ax, const u16* __restrict__ Awq, const u16* __restrict__ Awk, const u16* __restrict__ Awv,
                                                  const u16* __restrict__ Bx, const u16* __restrict__ Bw, char* __restrict__ ws) {
  extern __shared__ __attribute__((aligned(16))) char lds[];
  const int b = blockIdx.x, xcd = b & 7, j = b >> 3; const int rt = 2 * xcd + (j & 1), ct = j >> 1; const int row0 = rt * 128;
  u16* Qs = (u16*)(ws + WS_Q); u16* KV = (u16*)(ws + WS_KV); u16* QKV = (u16*)(ws + WS_QKV);
  if (ct >= 5) gemm_tile<2, 2>(lds, Bx, DM, row0, Bw, 1344, (ct - 5) * 128, 1344, nullptr, nullptr, QKV, nullptr);
  else if (ct >= 2) gemm_tile<0, 2>(lds, Ax, DM, row0, Awq, DM, (ct - 2) * 128, DM, nullptr, nullptr, Qs, nullptr);
  else if (ct == 1) gemm_tile<0, 1>(lds, Ax, DM, row0, Awq, DM, 384, DM, nullptr, nullptr, Qs, nullptr);
  else gemm_tile<1, 0>(lds, Ax, DM, row0, Awq, DM, 0, DM, Awk, Awv, Qs, KV);
}

__global__ __launch_bounds__(512, 2) void k3_out(const char* __restrict__ ws, const u16* __restrict__ Awo, const u16* __restrict__ Bwo, u16* __restrict__ out) {
  extern __shared__ __attribute__((aligned(16))) char lds[];
  const int b = blockIdx.x, xcd = b & 7, j = b >> 3; const int rt = 2 * xcd + (j & 1), ct = j >> 1; const int row0 = rt * 128;
  const u16* O = (const u16*)(ws + WS_O);
  const int part = ct >= 7 ? 1 : 0; const int c0 = (ct - 7 * part) * 64;
  gemm_tile<3, 1>(lds, O + part * 448, 896, row0, part ? Bwo : Awo, DM, c0, DM, nullptr, nullptr, out + part * 448, nullptr);
}

constexpr int A_STAGE = 16384;
constexpr int A_LDS_SIDE = 4 * A_STAGE;
constexpr int A_LDS_MRG = A_LDS_SIDE + 65536;
constexpr int A_LDS = A_LDS_MRG + 4 * 17 * 256;
constexpr float THR = 30.f;

DI float max16(const f32x16& s) {
  float a = __builtin_fmaxf(__builtin_fmaxf(s[0], s[1]), s[2]), b = __builtin_fmaxf(__builtin_fmaxf(s[3], s[4]), s[5]);
  a = __builtin_fmaxf(__builtin_fmaxf(a, s[6]), s[7]); b = __builtin_fmaxf(__builtin_fmaxf(b, s[8]), s[9]);
  a = __builtin_fmaxf(__builtin_fmaxf(a, s[10]), s[11]); b = __builtin_fmaxf(__builtin_fmaxf(b, s[12]), s[13]);
  a = __builtin_fmaxf(__builtin_fmaxf(a, s[14]), s[15]);
  return __builtin_fmaxf(a, b); }
DI float xhalf_max(float m) { auto rr = __builtin_amdgcn_permlane32_swap(__float_as_uint(m), __float_as_uint(m), false, false);
  return __builtin_fmaxf(__uint_as_float(rr[0]), __uint_as_float(rr[1])); }
DI float bf_round(float x) { return __uint_as_float(cvtpk(x, 0.f) << 16); }

DI float dot2(unsigned a, unsigned b, float c) { return __builtin_amdgcn_fdot2_f32_bf16(__builtin_bit_cast(bf16x2_t, a), __builtin_bit_cast(bf16x2_t, b), c, false); }
DI void lbar() { asm volatile("s_waitcnt lgkmcnt(0)\n\ts_barrier" ::: "memory"); }

__global__ __launch_bounds__(768, 1) void k2_attn(char* __restrict__ ws) {
  extern __shared__ __attribute__((aligned(16))) char lds[];
  const int tid = threadIdx.x, lane = tid & 63, r32 = lane & 31, hi = lane >> 5;
  const int wid = (int)sfl(tid >> 6), qg = wid & 3, kh = (wid >> 2) & 1;
  const int b = blockIdx.x, xcd = b & 7, j = b >> 3;
  const int rc = xcd * 8 + (j >> 2), hg = j & 3;
  const int head = hg * 4 + qg, row0 = rc * 32;
  const u16* Qs = (const u16*)(ws + WS_Q); const char* KV = ws + WS_KV; const char* QKV = ws + WS_QKV; u16* O = (u16*)(ws + WS_O);
  const unsigned lds0 = (unsigned)(uintptr_t)lds;
  const int brow0 = (xcd * 32 + j) * 8; const int bstart = brow0 >= 14 ? brow0 - 14 : 0;

  if (wid >= 8) {
    const char* kvsrc = KV + (size_t)(qg >> 1) * 131072 + (qg & 1) * 4096 + lane * 16;
    const unsigned kvdst = lds0 + qg * 4096;
    const char* sdsrc = QKV + (size_t)bstart * 2688 + qg * 2048 + lane * 16;
    auto issue_kv = [&](int s) { const unsigned d = kvdst + (s & 3) * A_STAGE;
#pragma unroll
      for (int i = 0; i < 4; ++i) glds16(kvsrc + (size_t)s * 8192 + i * 1024, d + i * 1024); };
    issue_kv(0); issue_kv(1); issue_kv(2); issue_kv(3);
    wait_bar<8>();
    for (int s = 1; s < 15; ++s) {
      if (s < 14) wait_bar<4>(); else wait_bar<0>();
      if (s <= 8) {
#pragma unroll
        for (int i = 0; i < 2; ++i) glds16(sdsrc + (s - 1) * 8192 + i * 1024, lds0 + A_LDS_SIDE + (s - 1) * 8192 + qg * 2048 + i * 1024); }
      if (s <= 12) issue_kv(s + 3);
    }
    {
    const int t = tid - 512, pr = t >> 1, hf = t & 1, rl = pr >> 4, hb = pr & 15; const int r = brow0 + rl;
    const char* img = lds + A_LDS_SIDE + hb * 56 + hf * 28;
    const unsigned* qv = (const unsigned*)(img + (r - bstart) * 2688);
    unsigned qu[7];
#pragma unroll
    for (int i = 0; i < 7; ++i) qu[i] = qv[i];
    float lg[8]; float m = -3.0e38f;
#pragma unroll
    for (int w = 0; w < 8; ++w) { const int jr = r - 2 * w; const int jc = jr >= 0 ? jr : bstart;
      const unsigned* kp = (const unsigned*)(img + (jc - bstart) * 2688 + 896); float a = 0.f;
#pragma unroll
      for (int i = 0; i < 7; ++i) a = dot2(qu[i], kp[i], a);
      a += __uint_as_float(__builtin_amdgcn_update_dpp(0, __float_as_uint(a), 0xB1, 0xF, 0xF, true));
      lg[w] = jr >= 0 ? a * SCALE : -1e9f; m = __builtin_fmaxf(m, lg[w]); }
    float l = 0.f;
#pragma unroll
    for (int w = 0; w < 8; ++w) { lg[w] = __builtin_amdgcn_exp2f((lg[w] - m) * LOG2E); l += lg[w]; }
    const float il = 1.0f / l; float oa[14];
#pragma unroll
    for (int i = 0; i < 14; ++i) oa[i] = 0.f;
#pragma unroll
    for (int w = 0; w < 8; w += 2) { const int j0 = r - 2 * w, j1 = j0 - 2; const int c0 = j0 >= 0 ? j0 : bstart, c1 = j1 >= 0 ? j1 : bstart;
      const unsigned pp = cvtpk(lg[w] * il, lg[w + 1] * il);
      const unsigned* v0 = (const unsigned*)(img + (c0 - bstart) * 2688 + 1792); const unsigned* v1 = (const unsigned*)(img + (c1 - bstart) * 2688 + 1792);
#pragma unroll
      for (int i = 0; i < 7; ++i) { const unsigned a = v0[i], bb = v1[i];
        oa[2 * i] = dot2(pp, __builtin_amdgcn_perm(bb, a, 0x05040100), oa[2 * i]); oa[2 * i + 1] = dot2(pp, __builtin_amdgcn_perm(bb, a, 0x07060302), oa[2 * i + 1]); } }
    unsigned* dst = (unsigned*)(O + (size_t)r * 896 + 448 + hb * 28 + hf * 14);
#pragma unroll
    for (int i = 0; i < 7; ++i) dst[i] = cvtpk(oa[2 * i], oa[2 * i + 1]);
    }
    lbar();
    return;
  }

  const u16* qp = Qs + ((size_t)head * S + row0 + r32) * 32 + hi * 8;
  const bf16x8 q0 = *(const bf16x8*)qp; u32x4 q1 = *(const u32x4*)(qp + 16);
  float kn2 = ((const float*)(ws + WS_KN))[lane];

  float qn2 = 0.f;
  { const u32x4 a = __builtin_bit_cast(u32x4, q0);
#pragma unroll
    for (int i = 0; i < 4; ++i) { float x = bflo(a[i]), y = bfhi(a[i]); qn2 = __builtin_fmaf(x, x, qn2); qn2 = __builtin_fmaf(y, y, qn2);
      x = bflo(q1[i]); y = bfhi(q1[i]); qn2 = __builtin_fmaf(x, x, qn2); qn2 = __builtin_fmaf(y, y, qn2); } }
  qn2 += __shfl_xor(qn2, 32, 64);
#pragma unroll
  for (int o = 32; o > 0; o >>= 1) kn2 = __builtin_fmaxf(kn2, __shfl_xor(kn2, o, 64));
  const float bound = __builtin_sqrtf(qn2 * kn2) * 1.01f + 1.0f;

  f32x16 o = f32x16{};
  const lds_cptr rd0 = (lds_cptr)lds + kh * 8192 + lane * 16;
  float mref, m0;
  auto set_ref = [&](float m) {
    const float mh = bf_round(m), ml = bf_round(m - mh); mref = mh + ml;
    const unsigned pk = cvtpk(-mh, -ml); if (hi) q1[2] = pk; };
#define Q1 __builtin_bit_cast(bf16x8, q1)
#define EXPK(S_, i) cvtpk(__builtin_amdgcn_exp2f(S_[i]), __builtin_amdgcn_exp2f(S_[i + 1]))

  lbar();
  bf16x8 ka0 = lds_rd128(rd0), ka1 = lds_rd128(rd0 + 1024);
  { f32x16 s = MFMA(ka0, q0, f32x16{}); s = MFMA(ka1, Q1, s); m0 = xhalf_max(max16(s)); }
  const bool fast = __all(bound - m0 <= 198.f);
  set_ref(fast ? __builtin_fmaxf(m0, bound - 108.f) : m0);

  if (fast) {
    f32x16 sc, sn; bf16x8 kb0, kb1, va0, va1, vb0, vb1;
    sc = MFMA(ka0, q0, f32x16{}); sc = MFMA(ka1, Q1, sc);
    ka0 = lds_rd128(rd0 + 4096); ka1 = lds_rd128(rd0 + 4096 + 1024); va0 = lds_rd128(rd0 + 2048); va1 = lds_rd128(rd0 + 3072);
#define ITER(SC_, SN_, K0, K1, V0, V1, NK0, NK1, NV0, NV1, PK_, PV_, HASK, HASV, HASN) do { \
      if (HASK) { NK0 = lds_rd128(PK_); NK1 = lds_rd128((PK_) + 1024); } \
      if (HASV) { NV0 = lds_rd128((PV_) + 2048); NV1 = lds_rd128((PV_) + 3072); } SB(); \
      u32x4 p0, p1; \
      p0[0] = EXPK(SC_, 0); p0[1] = EXPK(SC_, 2); SB(); \
      if (HASN) SN_ = MFMA(K0, q0, f32x16{}); SB(); \
      p0[2] = EXPK(SC_, 4); p0[3] = EXPK(SC_, 6); SB(); \
      o = MFMA(V0, __builtin_bit_cast(bf16x8, p0), o); SB(); \
      p1[0] = EXPK(SC_, 8); p1[1] = EXPK(SC_, 10); SB(); \
      if (HASN) SN_ = MFMA(K1, Q1, SN_); SB(); \
      p1[2] = EXPK(SC_, 12); p1[3] = EXPK(SC_, 14); SB(); \
      o = MFMA(V1, __builtin_bit_cast(bf16x8, p1), o); SB(); } while (0)
    for (int s = 0; s < 15; ++s) {
      const lds_cptr p = rd0 + (s & 3) * A_STAGE, pn = rd0 + ((s + 1) & 3) * A_STAGE;
      if (s > 0) lbar();
      ITER(sc, sn, ka0, ka1, va0, va1, kb0, kb1, vb0, vb1, pn, p + 4096, true, true, true);
      ITER(sn, sc, kb0, kb1, vb0, vb1, ka0, ka1, va0, va1, pn + 4096, pn, true, true, true);
    }
    { const lds_cptr p = rd0 + 3 * A_STAGE;
      ITER(sc, sn, ka0, ka1, va0, va1, kb0, kb1, vb0, vb1, p, p + 4096, false, true, true);
      ITER(sn, sc, kb0, kb1, vb0, vb1, ka0, ka1, va0, va1, p, p, false, false, false); }
  } else {
    auto tile = [&](lds_cptr p) {
      const bf16x8 kf0 = lds_rd128(p), kf1 = lds_rd128(p + 1024), vf0 = lds_rd128(p + 2048), vf1 = lds_rd128(p + 3072);
      f32x16 s = MFMA(kf0, q0, f32x16{}); s = MFMA(kf1, Q1, s);
      const float tm = max16(s);
      if (__any(tm > THR)) {
        const float old = mref; set_ref(mref + __builtin_fmaxf(xhalf_max(tm), 0.f)); const float dl = mref - old; const float al = __builtin_amdgcn_exp2f(-dl);
#pragma unroll
        for (int r = 0; r < 16; ++r) { s[r] -= dl; o[r] *= al; }
      }
      u32x4 p0, p1;
#pragma unroll
      for (int i = 0; i < 4; ++i) { p0[i] = EXPK(s, 2 * i); p1[i] = EXPK(s, 8 + 2 * i); }
      o = MFMA(vf0, __builtin_bit_cast(bf16x8, p0), o); o = MFMA(vf1, __builtin_bit_cast(bf16x8, p1), o);
    };
    for (int s = 0; s < 16; ++s) {
      const lds_cptr p = rd0 + (s & 3) * A_STAGE;
      if (s > 0 && s < 15) lbar();
      tile(p); tile(p + 4096);
    }
  }

  float* mrg = (float*)(lds + A_LDS_MRG) + qg * 17 * 64;
  if (kh == 1) {
#pragma unroll
    for (int r = 0; r < 16; ++r) mrg[r * 64 + lane] = o[r];
    mrg[16 * 64 + lane] = -mref;
  }
  lbar();
  if (kh == 0) {
    const float nb = mrg[16 * 64 + lane], na = -mref;
    const float nm = __builtin_fminf(na, nb);
    const float fa = __builtin_amdgcn_exp2f(nm - na), fb = __builtin_amdgcn_exp2f(nm - nb);
#pragma unroll
    for (int r = 0; r < 16; ++r) o[r] = o[r] * fa + mrg[r * 64 + lane] * fb;
    const float l = __shfl(o[12], r32 + 32, 64);
    const float il = 1.0f / l;
    u16* dst = O + (size_t)(row0 + r32) * 896 + head * 28 + 4 * hi;
#pragma unroll
    for (int g = 0; g < 4; ++g) if (!(g == 3 && hi == 1))
      *(u32x2*)(dst + 8 * g) = u32x2{cvtpk(o[4 * g] * il, o[4 * g + 1] * il), cvtpk(o[4 * g + 2] * il, o[4 * g + 3] * il)};

  }
}

extern "C" void kernel_launch(void* const* d_in, const int* in_sizes, int n_in, void* d_out, int out_size, void* d_ws, size_t ws_size, hipStream_t stream) {
  const u16 *Ax = (const u16*)d_in[0], *Awq = (const u16*)d_in[1], *Awk = (const u16*)d_in[2], *Awv = (const u16*)d_in[3], *Awo = (const u16*)d_in[4];
  const u16 *Bx = (const u16*)d_in[5], *Bw = (const u16*)d_in[6], *Bwo = (const u16*)d_in[7];
  char* ws = (char*)d_ws;
  (void)hipFuncSetAttribute((const void*)k1_proj, hipFuncAttributeMaxDynamicSharedMemorySize, K1_LDS);
  (void)hipFuncSetAttribute((const void*)k2_attn, hipFuncAttributeMaxDynamicSharedMemorySize, A_LDS);
  (void)hipFuncSetAttribute((const void*)k3_out, hipFuncAttributeMaxDynamicSharedMemorySize, K3_LDS);
  k1_proj<<<256, 512, K1_LDS, stream>>>(Ax, Awq, Awk, Awv, Bx, Bw, ws);
  k2_attn<<<256, 768, A_LDS, stream>>>(ws);
  k3_out<<<224, 512, K3_LDS, stream>>>(ws, Awo, Bwo, (u16*)d_out);
}
```

```cpp
#include <hip/hip_runtime.h>
#include <stdint.h>

typedef unsigned short u16;
using bf16x8 = __attribute__((ext_vector_type(8))) short;
using s16x4  = __attribute__((ext_vector_type(4))) short;
using f32x16 = __attribute__((ext_vector_type(16))) float;
using u32x4  = __attribute__((ext_vector_type(4))) unsigned;
using u32x2  = __attribute__((ext_vector_type(2))) unsigned;
typedef __attribute__((address_space(3))) const char* lds_cptr;
typedef __attribute__((address_space(3))) char* lds_ptr;
typedef short v4i16_t __attribute__((ext_vector_type(4)));
typedef __bf16 bf16x2_t __attribute__((ext_vector_type(2)));
typedef float f32x2_t __attribute__((ext_vector_type(2)));
#define DI __device__ __forceinline__
#define MFMA(a, b, c) __builtin_amdgcn_mfma_f32_32x32x16_bf16((a), (b), (c), 0, 0, 0)
#define SB() __builtin_amdgcn_sched_barrier(0)

constexpr int S = 2048, DM = 448, NH = 16, DH = 28;
constexpr float SCALE = 0.18898223650461362f;
constexpr float LOG2E = 1.4426950408889634f;
constexpr float QSCALE = SCALE * LOG2E;

constexpr size_t WS_Q   = 0;
constexpr size_t WS_KV  = WS_Q + (size_t)NH * S * 32 * 2;
constexpr size_t WS_KN  = WS_KV + (size_t)64 * 4096;
constexpr size_t WS_QKV = WS_KN + 256;
constexpr size_t WS_O   = WS_QKV + (size_t)S * 1344 * 2 + 8192;

DI unsigned cvtpk(float lo, float hi) { f32x2_t v = {lo, hi}; return __builtin_bit_cast(unsigned, __builtin_convertvector(v, bf16x2_t)); }
DI void glds16(const void* g, unsigned lds_base) {
  unsigned sv; asm volatile("s_mov_b32 %0, m0\n\ts_mov_b32 m0, %2\n\ts_nop 0\n\tglobal_load_lds_dwordx4 %1, off\n\ts_mov_b32 m0, %0"
                            : "=&s"(sv) : "v"(g), "s"(lds_base) : "memory"); }
template <int N> DI void wait_bar() { asm volatile("s_waitcnt vmcnt(%0) lgkmcnt(0)\n\ts_barrier" :: "i"(N) : "memory"); }
DI bf16x8 lds_rd128(lds_cptr p) { return *(const __attribute__((address_space(3))) bf16x8*)p; }
DI s16x4 lds_tr(lds_cptr p) { return __builtin_bit_cast(s16x4, __builtin_amdgcn_ds_read_tr16_b64_v4i16((__attribute__((address_space(3))) v4i16_t*)p)); }
DI unsigned sfl(unsigned x) { return (unsigned)__builtin_amdgcn_readfirstlane((int)x); }
template <int I> struct IC { static constexpr int value = I; };
template <int I, int N, class F> DI void static_for(F&& f) { if constexpr (I < N) { f(IC<I>{}); static_for<I + 1, N>(f); } }
DI float bflo(unsigned u) { return __uint_as_float(u << 16); }
DI float bfhi(unsigned u) { return __uint_as_float(u & 0xffff0000u); }

constexpr int G_NBUF = 4;
template <int NPP> struct GemmCfg { static constexpr int STAGE_BYTES = 16384 + NPP * 8192; static constexpr int RING = STAGE_BYTES * G_NBUF; };
constexpr int K1_LDS = GemmCfg<2>::RING;
static_assert(GemmCfg<0>::RING + 2 * 448 * 64 <= K1_LDS, "");
constexpr int K3_LDS = GemmCfg<1>::RING;

template <int MODE, int NPP>
DI void gemm_tile(char* lds, const u16* __restrict__ X, int ldx, int row0, const u16* __restrict__ W, int ldw, int col0, int ncols,
                  const u16* __restrict__ Wk, const u16* __restrict__ Wv, u16* __restrict__ out0, u16* __restrict__ out1) {
  constexpr int SBY = GemmCfg<NPP>::STAGE_BYTES;
  constexpr int NPW = (MODE == 1) ? 1 : NPP;
  constexpr int L = 2 + NPP;
  const int tid = threadIdx.x, lane = tid & 63, r32 = lane & 31, hi = lane >> 5;
  const int wid = (int)sfl(tid >> 6);
  const unsigned lds0 = (unsigned)(uintptr_t)lds;
  const u16* xsrc[2];
#pragma unroll
  for (int i = 0; i < 2; ++i) { const int row = (2 * wid + i) * 8 + (lane >> 3); const int c = (lane & 7) ^ ((row >> 1) & 7);
    xsrc[i] = X + (size_t)(row0 + row) * ldx + c * 8; }
  const u16* wsrc[NPP > 0 ? NPP : 1];
#pragma unroll
  for (int i = 0; i < NPP; ++i) { int pc = col0 + i * 64; if (pc > ncols - 64) pc = ncols - 64;
    const int k = wid * 8 + (lane >> 3); const int c = (lane & 7) ^ (((k >> 1) & 1) << 2);
    wsrc[i] = W + (size_t)k * ldw + pc + c * 8; }

  auto issue = [&](int s) {
    const unsigned base = lds0 + (unsigned)((s % G_NBUF) * SBY);
    glds16(xsrc[0] + s * 64, sfl(base + (2 * wid) * 1024));
    glds16(xsrc[1] + s * 64, sfl(base + (2 * wid + 1) * 1024));
#pragma unroll
    for (int i = 0; i < NPP; ++i) glds16(wsrc[i] + (size_t)s * 64 * ldw, sfl(base + 16384 + i * 8192 + wid * 1024));
  };

  const int wr = wid & 3, wc = wid >> 2;
  const int xrow = wr * 32 + r32; const int xsw = (xrow >> 1) & 7;
  const int g16 = lane >> 4, i16 = lane & 15, tq = i16 >> 2;
  const int toff128 = (8 * (g16 >> 1) + tq) * 128 + (g16 & 1) * 32 + (i16 & 3) * 8; const int tsw = (tq >> 1) & 1;
  const int toff64  = (8 * (g16 >> 1) + tq) * 64 + (g16 & 1) * 32 + (i16 & 3) * 8;

  f32x16 acc[NPW];
#pragma unroll
  for (int p = 0; p < NPW; ++p) acc[p] = f32x16{};

  u32x2 kvreg[13];
  if constexpr (MODE == 1) {
#pragma unroll
    for (int i = 0; i < 13; ++i) { int e = tid + i * 512; if (e > 6271) e = 6271; const int m = e >= 3136; const int e2 = e - m * 3136;
      kvreg[i] = *(const u32x2*)((m ? Wv : Wk) + e2 * 4); }
  }
  issue(0); issue(1); issue(2);
  if constexpr (MODE == 1) {
#pragma unroll
    for (int i = 0; i < 13; ++i) { int e = tid + i * 512; if (e > 6271) e = 6271; const int m = e >= 3136; const int e2 = e - m * 3136; const int k = e2 / 7, c = e2 - k * 7;
      *(u32x2*)(lds + GemmCfg<0>::RING + m * 28672 + k * 64 + c * 8) = kvreg[i]; }
  }
  auto compute = [&](int s) {
    const lds_cptr b = (lds_cptr)lds + (s % G_NBUF) * SBY;
#pragma unroll
    for (int ks = 0; ks < 4; ++ks) {
      const bf16x8 xf = lds_rd128(b + xrow * 128 + (((2 * ks + hi) ^ xsw) * 16));
#pragma unroll
      for (int p = 0; p < NPW; ++p) {
        lds_cptr a; int step;
        if constexpr (MODE == 1) { a = (lds_cptr)lds + GemmCfg<0>::RING + wc * 28672 + (s * 64 + ks * 16) * 64 + toff64; step = 256; }
        else if constexpr (NPP == 2) { a = b + 16384 + wc * 8192 + ks * 2048 + toff128 + ((p ^ tsw) * 64); step = 512; }
        else { a = b + 16384 + ks * 2048 + toff128 + ((wc ^ tsw) * 64); step = 512; }
        const s16x4 lo = lds_tr(a), hh = lds_tr(a + step);
        const bf16x8 wf = __builtin_shufflevector(lo, hh, 0, 1, 2, 3, 4, 5, 6, 7);
        if constexpr (MODE == 1) { const bf16x8 fa = wc ? xf : wf, fb = wc ? wf : xf; acc[p] = MFMA(fa, fb, acc[p]); }
        else acc[p] = MFMA(wf, xf, acc[p]);
      }
    }
  };
#pragma unroll 1
  for (int s = 0; s < 7; ++s) {
    if (s < 5) wait_bar<2 * L>(); else if (s == 5) wait_bar<L>(); else wait_bar<0>();
    if (s < 4) issue(s + 3);
    compute(s);
  }

  const int row = row0 + xrow;
  if constexpr (MODE != 1) {
    constexpr int RB = NPW * 64 + 16;
    constexpr float OS = MODE == 0 ? QSCALE : 1.0f;
    __syncthreads();
    char* st = lds + wid * (32 * RB);
#pragma unroll
    for (int p = 0; p < NPW; ++p)
#pragma unroll
      for (int g = 0; g < 4; ++g)
        *(u32x2*)(st + r32 * RB + p * 64 + g * 16 + hi * 8) = u32x2{cvtpk(acc[p][4 * g] * OS, acc[p][4 * g + 1] * OS), cvtpk(acc[p][4 * g + 2] * OS, acc[p][4 * g + 3] * OS)};
    asm volatile("s_waitcnt lgkmcnt(0)" ::: "memory");
    const int cbase = col0 + wc * NPW * 32;
    if constexpr (MODE == 0) {
      constexpr int GPR = NPW * 8;
      const int cg = lane % GPR; const int c = cbase + cg * 4; const int head = c / 28, d = c - head * 28;
#pragma unroll
      for (int i = 0; i < 32 * GPR / 64; ++i) { const int rr = i * (64 / GPR) + lane / GPR;
        const u32x2 v = *(const u32x2*)(st + rr * RB + cg * 8);
        u16* dst = out0 + ((size_t)head * S + row0 + wr * 32 + rr) * 32 + d;
        if (d == 24) *(u32x4*)dst = u32x4{v[0], v[1], 0u, 0u}; else *(u32x2*)dst = v; }
    } else {
      constexpr int LPR = NPW * 4;
      const int ld = MODE == 2 ? 1344 : 896;
      if (MODE == 3 || cbase < 1344) {
#pragma unroll
        for (int i = 0; i < 32 * LPR / 64; ++i) { const int rr = i * (64 / LPR) + lane / LPR, ch = lane % LPR;
          *(u32x4*)(out0 + (size_t)(row0 + wr * 32 + rr) * ld + cbase + ch * 8) = *(const u32x4*)(st + rr * RB + ch * 16); }
      }
    }
  } else {
#pragma unroll
    for (int p = 0; p < NPW; ++p) {
      const int panel = wc * NPW + p;
      if (MODE == 1) {
        const int tile = (row0 >> 5) + wr; u16* kv = out1 + (size_t)tile * 2048;
        if (wc == 0) {
          float ss = 0.f;
#pragma unroll
          for (int g = 0; g < 4; ++g) { u32x2 v;
            if (g == 3 && hi == 1) v = u32x2{0x3F803F80u, 0u};
            else { v = u32x2{cvtpk(acc[p][4 * g], acc[p][4 * g + 1]), cvtpk(acc[p][4 * g + 2], acc[p][4 * g + 3])};
#pragma unroll
              for (int i = 0; i < 2; ++i) { const float a = bflo(v[i]), b = bfhi(v[i]); ss = __builtin_fmaf(a, a, ss); ss = __builtin_fmaf(b, b, ss); } }
            *(u32x2*)(kv + (g >> 1) * 512 + (r32 + 32 * (g & 1)) * 8 + 4 * hi) = v; }
          ss += __shfl_xor(ss, 32, 64);
#pragma unroll
          for (int o = 16; o > 0; o >>= 1) ss = __builtin_fmaxf(ss, __shfl_xor(ss, o, 64));
          if (lane == 0) ((float*)((char*)out1 + (WS_KN - WS_KV)))[tile] = ss;
        } else {
#pragma unroll
          for (int g = 0; g < 4; ++g) { u32x2 v = u32x2{cvtpk(acc[p][4 * g], acc[p][4 * g + 1]), cvtpk(acc[p][4 * g + 2], acc[p][4 * g + 3])};
            if (r32 == 28) v = u32x2{0x3F803F80u, 0x3F803F80u}; else if (r32 > 28) v = u32x2{0u, 0u};
            *(u32x2*)(kv + 1024 + (g >> 1) * 512 + lane * 8 + 4 * (g & 1)) = v; }
        }
      } else {
#pragma unroll
        for (int g = 0; g < 4; ++g) { const int c = col0 + panel * 32 + 8 * g + 4 * hi; const int head = c / 28, d = c - head * 28;
          u32x2 v = u32x2{cvtpk(acc[p][4 * g] * QSCALE, acc[p][4 * g + 1] * QSCALE), cvtpk(acc[p][4 * g + 2] * QSCALE, acc[p][4 * g + 3] * QSCALE)};
          u16* dst = out0 + ((size_t)head * S + row) * 32 + d;
          if (d == 24) *(u32x4*)dst = u32x4{v[0], v[1], 0u, 0u}; else *(u32x2*)dst = v; }
      }
    }
  }
}

__global__ __launch_bounds__(512, 2) void k1_proj(const u16* __restrict__ Ax, const u16* __restrict__ Awq, const u16* __restrict__ Awk, const u16* __restrict__ Awv,
                                                  const u16* __restrict__ Bx, const u16* __restrict__ Bw, char* __restrict__ ws) {
  extern __shared__ __attribute__((aligned(16))) char lds[];
  const int b = blockIdx.x, xcd = b & 7, j = b >> 3; const int rt = 2 * xcd + (j & 1), ct = j >> 1; const int row0 = rt * 128;
  u16* Qs = (u16*)(ws + WS_Q); u16* KV = (u16*)(ws + WS_KV); u16* QKV = (u16*)(ws + WS_QKV);
  if (ct >= 5) gemm_tile<2, 2>(lds, Bx, DM, row0, Bw, 1344, (ct - 5) * 128, 1344, nullptr, nullptr, QKV, nullptr);
  else if (ct >= 2) gemm_tile<0, 2>(lds, Ax, DM, row0, Awq, DM, (ct - 2) * 128, DM, nullptr, nullptr, Qs, nullptr);
  else if (ct == 1) gemm_tile<0, 1>(lds, Ax, DM, row0, Awq, DM, 384, DM, nullptr, nullptr, Qs, nullptr);
  else gemm_tile<1, 0>(lds, Ax, DM, row0, Awq, DM, 0, DM, Awk, Awv, Qs, KV);
}

__global__ __launch_bounds__(512, 2) void k3_out(const char* __restrict__ ws, const u16* __restrict__ Awo, const u16* __restrict__ Bwo, u16* __restrict__ out) {
  extern __shared__ __attribute__((aligned(16))) char lds[];
  const int b = blockIdx.x, xcd = b & 7, j = b >> 3; const int rt = 2 * xcd + (j & 1), ct = j >> 1; const int row0 = rt * 128;
  const u16* O = (const u16*)(ws + WS_O);
  const int part = ct >= 7 ? 1 : 0; const int c0 = (ct - 7 * part) * 64;
  gemm_tile<3, 1>(lds, O + part * 448, 896, row0, part ? Bwo : Awo, DM, c0, DM, nullptr, nullptr, out + part * 448, nullptr);
}

constexpr int A_STAGE = 16384;
constexpr int A_LDS_SIDE = 4 * A_STAGE;
constexpr int A_LDS_MRG = A_LDS_SIDE + 65536;
constexpr int A_LDS = A_LDS_MRG + 4 * 17 * 256;
constexpr float THR = 30.f;

DI float max16(const f32x16& s) {
  float a = __builtin_fmaxf(__builtin_fmaxf(s[0], s[1]), s[2]), b = __builtin_fmaxf(__builtin_fmaxf(s[3], s[4]), s[5]);
  a = __builtin_fmaxf(__builtin_fmaxf(a, s[6]), s[7]); b = __builtin_fmaxf(__builtin_fmaxf(b, s[8]), s[9]);
  a = __builtin_fmaxf(__builtin_fmaxf(a, s[10]), s[11]); b = __builtin_fmaxf(__builtin_fmaxf(b, s[12]), s[13]);
  a = __builtin_fmaxf(__builtin_fmaxf(a, s[14]), s[15]);
  return __builtin_fmaxf(a, b); }
DI float xhalf_max(float m) { auto rr = __builtin_amdgcn_permlane32_swap(__float_as_uint(m), __float_as_uint(m), false, false);
  return __builtin_fmaxf(__uint_as_float(rr[0]), __uint_as_float(rr[1])); }
DI float bf_round(float x) { return __uint_as_float(cvtpk(x, 0.f) << 16); }

DI float dot2(unsigned a, unsigned b, float c) { return __builtin_amdgcn_fdot2_f32_bf16(__builtin_bit_cast(bf16x2_t, a), __builtin_bit_cast(bf16x2_t, b), c, false); }
DI void lbar() { asm volatile("s_waitcnt lgkmcnt(0)\n\ts_barrier" ::: "memory"); }

__global__ __launch_bounds__(768, 1) void k2_attn(char* __restrict__ ws) {
  extern __shared__ __attribute__((aligned(16))) char lds[];
  const int tid = threadIdx.x, lane = tid & 63, r32 = lane & 31, hi = lane >> 5;
  const int wid = (int)sfl(tid >> 6), qg = wid & 3, kh = (wid >> 2) & 1;
  const int b = blockIdx.x, xcd = b & 7, j = b >> 3;
  const int rc = xcd * 8 + (j >> 2), hg = j & 3;
  const int head = hg * 4 + qg, row0 = rc * 32;
  const u16* Qs = (const u16*)(ws + WS_Q); const char* KV = ws + WS_KV; const char* QKV = ws + WS_QKV; u16* O = (u16*)(ws + WS_O);
  const unsigned lds0 = (unsigned)(uintptr_t)lds;
  const int brow0 = (xcd * 32 + j) * 8; const int bstart = brow0 >= 14 ? brow0 - 14 : 0;

  if (wid >= 8) {
    const char* kvsrc = KV + (size_t)(qg >> 1) * 131072 + (qg & 1) * 4096 + lane * 16;
    const unsigned kvdst = lds0 + qg * 4096;
    const char* sdsrc = QKV + (size_t)bstart * 2688 + qg * 2048 + lane * 16;
    auto issue_kv = [&](int s) { const unsigned d = kvdst + (s & 3) * A_STAGE;
#pragma unroll
      for (int i = 0; i < 4; ++i) glds16(kvsrc + (size_t)s * 8192 + i * 1024, d + i * 1024); };
    issue_kv(0); issue_kv(1); issue_kv(2); issue_kv(3);
    wait_bar<8>();
    for (int s = 1; s < 15; ++s) {
      if (s < 14) wait_bar<4>(); else wait_bar<0>();
      if (s <= 8) {
#pragma unroll
        for (int i = 0; i < 2; ++i) glds16(sdsrc + (s - 1) * 8192 + i * 1024, lds0 + A_LDS_SIDE + (s - 1) * 8192 + qg * 2048 + i * 1024); }
      if (s <= 12) issue_kv(s + 3);
    }
    {
    const int t = tid - 512, pr = t >> 1, hf = t & 1, rl = pr >> 4, hb = pr & 15; const int r = brow0 + rl;
    const char* img = lds + A_LDS_SIDE + hb * 56 + hf * 28;
    const unsigned* qv = (const unsigned*)(img + (r - bstart) * 2688);
    unsigned qu[7];
#pragma unroll
    for (int i = 0; i < 7; ++i) qu[i] = qv[i];
    float lg[8]; float m = -3.0e38f;
#pragma unroll
    for (int w = 0; w < 8; ++w) { const int jr = r - 2 * w; const int jc = jr >= 0 ? jr : bstart;
      const unsigned* kp = (const unsigned*)(img + (jc - bstart) * 2688 + 896); float a = 0.f;
#pragma unroll
      for (int i = 0; i < 7; ++i) a = dot2(qu[i], kp[i], a);
      a += __uint_as_float(__builtin_amdgcn_update_dpp(0, __float_as_uint(a), 0xB1, 0xF, 0xF, true));
      lg[w] = jr >= 0 ? a * SCALE : -1e9f; m = __builtin_fmaxf(m, lg[w]); }
    float l = 0.f;
#pragma unroll
    for (int w = 0; w < 8; ++w) { lg[w] = __builtin_amdgcn_exp2f((lg[w] - m) * LOG2E); l += lg[w]; }
    const float il = 1.0f / l; float oa[14];
#pragma unroll
    for (int i = 0; i < 14; ++i) oa[i] = 0.f;
#pragma unroll
    for (int w = 0; w < 8; w += 2) { const int j0 = r - 2 * w, j1 = j0 - 2; const int c0 = j0 >= 0 ? j0 : bstart, c1 = j1 >= 0 ? j1 : bstart;
      const unsigned pp = cvtpk(lg[w] * il, lg[w + 1] * il);
      const unsigned* v0 = (const unsigned*)(img + (c0 - bstart) * 2688 + 1792); const unsigned* v1 = (const unsigned*)(img + (c1 - bstart) * 2688 + 1792);
#pragma unroll
      for (int i = 0; i < 7; ++i) { const unsigned a = v0[i], bb = v1[i];
        oa[2 * i] = dot2(pp, __builtin_amdgcn_perm(bb, a, 0x05040100), oa[2 * i]); oa[2 * i + 1] = dot2(pp, __builtin_amdgcn_perm(bb, a, 0x07060302), oa[2 * i + 1]); } }
    unsigned* dst = (unsigned*)(O + (size_t)r * 896 + 448 + hb * 28 + hf * 14);
#pragma unroll
    for (int i = 0; i < 7; ++i) dst[i] = cvtpk(oa[2 * i], oa[2 * i + 1]);
    }
    lbar();
    return;
  }

  const u16* qp = Qs + ((size_t)head * S + row0 + r32) * 32 + hi * 8;
  const bf16x8 q0 = *(const bf16x8*)qp; u32x4 q1 = *(const u32x4*)(qp + 16);
  float kn2 = ((const float*)(ws + WS_KN))[lane];

  float qn2 = 0.f;
  { const u32x4 a = __builtin_bit_cast(u32x4, q0);
#pragma unroll
    for (int i = 0; i < 4; ++i) { float x = bflo(a[i]), y = bfhi(a[i]); qn2 = __builtin_fmaf(x, x, qn2); qn2 = __builtin_fmaf(y, y, qn2);
      x = bflo(q1[i]); y = bfhi(q1[i]); qn2 = __builtin_fmaf(x, x, qn2); qn2 = __builtin_fmaf(y, y, qn2); } }
  qn2 += __shfl_xor(qn2, 32, 64);
#pragma unroll
  for (int o = 32; o > 0; o >>= 1) kn2 = __builtin_fmaxf(kn2, __shfl_xor(kn2, o, 64));
  const float bound = __builtin_sqrtf(qn2 * kn2) * 1.01f + 1.0f;

  f32x16 o = f32x16{};
  const lds_cptr rd0 = (lds_cptr)lds + kh * 8192 + lane * 16;
  float mref, m0;
  auto set_ref = [&](float m) {
    const float mh = bf_round(m), ml = bf_round(m - mh); mref = mh + ml;
    const unsigned pk = cvtpk(-mh, -ml); if (hi) q1[2] = pk; };
#define Q1 __builtin_bit_cast(bf16x8, q1)
#define EXPK(S_, i) cvtpk(__builtin_amdgcn_exp2f(S_[i]), __builtin_amdgcn_exp2f(S_[i + 1]))

  lbar();
  bf16x8 ka0 = lds_rd128(rd0), ka1 = lds_rd128(rd0 + 1024);
  { f32x16 s = MFMA(ka0, q0, f32x16{}); s = MFMA(ka1, Q1, s); m0 = xhalf_max(max16(s)); }
  const bool fast = __all(bound - m0 <= 198.f);
  set_ref(fast ? __builtin_fmaxf(m0, bound - 108.f) : m0);

  if (fast) {
    f32x16 sc, sn; bf16x8 kb0, kb1, va0, va1, vb0, vb1;
    sc = MFMA(ka0, q0, f32x16{}); sc = MFMA(ka1, Q1, sc);
    ka0 = lds_rd128(rd0 + 4096); ka1 = lds_rd128(rd0 + 4096 + 1024); va0 = lds_rd128(rd0 + 2048); va1 = lds_rd128(rd0 + 3072);
#define ITER(SC_, SN_, K0, K1, V0, V1, NK0, NK1, NV0, NV1, PK_, PV_, HASK, HASV, HASN) do { \
      if (HASK) { NK0 = lds_rd128(PK_); NK1 = lds_rd128((PK_) + 1024); } \
      if (HASV) { NV0 = lds_rd128((PV_) + 2048); NV1 = lds_rd128((PV_) + 3072); } SB(); \
      u32x4 p0, p1; \
      p0[0] = EXPK(SC_, 0); p0[1] = EXPK(SC_, 2); SB(); \
      if (HASN) SN_ = MFMA(K0, q0, f32x16{}); SB(); \
      p0[2] = EXPK(SC_, 4); p0[3] = EXPK(SC_, 6); SB(); \
      o = MFMA(V0, __builtin_bit_cast(bf16x8, p0), o); SB(); \
      p1[0] = EXPK(SC_, 8); p1[1] = EXPK(SC_, 10); SB(); \
      if (HASN) SN_ = MFMA(K1, Q1, SN_); SB(); \
      p1[2] = EXPK(SC_, 12); p1[3] = EXPK(SC_, 14); SB(); \
      o = MFMA(V1, __builtin_bit_cast(bf16x8, p1), o); SB(); } while (0)
    for (int s = 0; s < 15; ++s) {
      const lds_cptr p = rd0 + (s & 3) * A_STAGE, pn = rd0 + ((s + 1) & 3) * A_STAGE;
      if (s > 0) lbar();
      ITER(sc, sn, ka0, ka1, va0, va1, kb0, kb1, vb0, vb1, pn, p + 4096, true, true, true);
      ITER(sn, sc, kb0, kb1, vb0, vb1, ka0, ka1, va0, va1, pn + 4096, pn, true, true, true);
    }
    { const lds_cptr p = rd0 + 3 * A_STAGE;
      ITER(sc, sn, ka0, ka1, va0, va1, kb0, kb1, vb0, vb1, p, p + 4096, false, true, true);
      ITER(sn, sc, kb0, kb1, vb0, vb1, ka0, ka1, va0, va1, p, p, false, false, false); }
  } else {
    auto tile = [&](lds_cptr p) {
      const bf16x8 kf0 = lds_rd128(p), kf1 = lds_rd128(p + 1024), vf0 = lds_rd128(p + 2048), vf1 = lds_rd128(p + 3072);
      f32x16 s = MFMA(kf0, q0, f32x16{}); s = MFMA(kf1, Q1, s);
      const float tm = max16(s);
      if (__any(tm > THR)) {
        const float old = mref; set_ref(mref + __builtin_fmaxf(xhalf_max(tm), 0.f)); const float dl = mref - old; const float al = __builtin_amdgcn_exp2f(-dl);
#pragma unroll
        for (int r = 0; r < 16; ++r) { s[r] -= dl; o[r] *= al; }
      }
      u32x4 p0, p1;
#pragma unroll
      for (int i = 0; i < 4; ++i) { p0[i] = EXPK(s, 2 * i); p1[i] = EXPK(s, 8 + 2 * i); }
      o = MFMA(vf0, __builtin_bit_cast(bf16x8, p0), o); o = MFMA(vf1, __builtin_bit_cast(bf16x8, p1), o);
    };
    for (int s = 0; s < 16; ++s) {
      const lds_cptr p = rd0 + (s & 3) * A_STAGE;
      if (s > 0 && s < 15) lbar();
      tile(p); tile(p + 4096);
    }
  }

  float* mrg = (float*)(lds + A_LDS_MRG) + qg * 17 * 64;
  if (kh == 1) {
#pragma unroll
    for (int r = 0; r < 16; ++r) mrg[r * 64 + lane] = o[r];
    mrg[16 * 64 + lane] = -mref;
  }
  lbar();
  if (kh == 0) {
    const float nb = mrg[16 * 64 + lane], na = -mref;
    const float nm = __builtin_fminf(na, nb);
    const float fa = __builtin_amdgcn_exp2f(nm - na), fb = __builtin_amdgcn_exp2f(nm - nb);
#pragma unroll
    for (int r = 0; r < 16; ++r) o[r] = o[r] * fa + mrg[r * 64 + lane] * fb;
    const float l = __shfl(o[12], r32 + 32, 64);
    const float il = 1.0f / l;
    u16* dst = O + (size_t)(row0 + r32) * 896 + head * 28 + 4 * hi;
#pragma unroll
    for (int g = 0; g < 4; ++g) if (!(g == 3 && hi == 1))
      *(u32x2*)(dst + 8 * g) = u32x2{cvtpk(o[4 * g] * il, o[4 * g + 1] * il), cvtpk(o[4 * g + 2] * il, o[4 * g + 3] * il)};

  }
}

extern "C" void kernel_launch(void* const* d_in, const int* in_sizes, int n_in, void* d_out, int out_size, void* d_ws, size_t ws_size, hipStream_t stream) {
  const u16 *Ax = (const u16*)d_in[0], *Awq = (const u16*)d_in[1], *Awk = (const u16*)d_in[2], *Awv = (const u16*)d_in[3], *Awo = (const u16*)d_in[4];
  const u16 *Bx = (const u16*)d_in[5], *Bw = (const u16*)d_in[6], *Bwo = (const u16*)d_in[7];
  char* ws = (char*)d_ws;
  (void)hipFuncSetAttribute((const void*)k1_proj, hipFuncAttributeMaxDynamicSharedMemorySize, K1_LDS);
  (void)hipFuncSetAttribute((const void*)k2_attn, hipFuncAttributeMaxDynamicSharedMemorySize, A_LDS);
  (void)hipFuncSetAttribute((const void*)k3_out, hipFuncAttributeMaxDynamicSharedMemorySize, K3_LDS);
  k1_proj<<<256, 512, K1_LDS, stream>>>(Ax, Awq, Awk, Awv, Bx, Bw, ws);
  k2_attn<<<256, 768, A_LDS, stream>>>(ws);
  k3_out<<<224, 512, K3_LDS, stream>>>(ws, Awo, Bwo, (u16*)d_out);
}
```
